```python
import torch

def _softmax(x, axis=-1):
    return torch.softmax(x, dim=axis)
def _gelu(x):
    return torch.nn.functional.gelu(x, approximate="tanh")
def _silu(x):
    return torch.nn.functional.silu(x)
def _sigmoid(x):
    return torch.sigmoid(x)
def _layernorm(x, axis=-1, eps=1e-5):
    m = x.mean(dim=axis, keepdim=True)
    v = x.var(dim=axis, keepdim=True, unbiased=False)
    return (x - m) / torch.sqrt(v + eps)
def _scatter_add(tgt, idx, src, axis=0):
    return tgt.clone().index_add_(axis, idx, src)
def _segment_sum(src, seg, n):
    out = torch.zeros((n,) + src.shape[1:], dtype=src.dtype)
    return out.index_add_(0, seg, src)
def _pad(x, pw):
    flat = tuple(v for pair in reversed(pw) for v in pair)
    return torch.nn.functional.pad(x, flat)

kernel_name = 'gated_mix__ssm_conv_h3__dense_activation_gelu__prompt'

def setup_inputs():

    _w = (lambda a: (lambda t: t.to(torch.bfloat16) if t.is_floating_point() else t)(torch.as_tensor(a)))
    def _A_setup():
        B, S, D = 1, 2048, 640
        ks = 4
        return {
            "q": _w((lambda *s: torch.randn(*s, dtype=torch.bfloat16) * 0.05)(B, S, D)),
            "k": _w((lambda *s: torch.randn(*s, dtype=torch.bfloat16) * 0.2)(B, S, D)),
            "v": _w((lambda *s: torch.randn(*s, dtype=torch.bfloat16) * 0.2)(B, S, D)),
            "shift_k": _w((lambda *s: torch.randn(*s, dtype=torch.bfloat16) * 0.2)(D, ks) * 0.5),
            "ssm_k": _w((lambda *s: torch.randn(*s, dtype=torch.bfloat16) * 0.8)(D, S) * (1.0 / S ** 0.5)),
            "ssm_D": _w((lambda *s: torch.randn(*s, dtype=torch.bfloat16) * 0.2)(D)),
        }
    _A_in = _A_setup()

    def _B_setup():
        B, S, D, F = 1, 2048, 640, 1536
        return {
            "x": _w((lambda *s: torch.randn(*s, dtype=torch.bfloat16) * 0.05)(B, S, D)),
            "w1": _w((lambda *s: torch.randn(*s, dtype=torch.bfloat16) * 3.2)(D, F) * (D ** -0.5)),
            "b1": _w((lambda *s: torch.randn(*s, dtype=torch.bfloat16) * 3.2)(F) * 0.01),
            "w2": _w((lambda *s: torch.randn(*s, dtype=torch.bfloat16) * 0.05)(F, D) * (F ** -0.5)),
            "b2": _w((lambda *s: torch.randn(*s, dtype=torch.bfloat16) * 0.1)(D) * 0.01),
        }
    _B_in = _B_setup()

    return {
        "A_q": _A_in["q"],
        "A_k": _A_in["k"],
        "A_v": _A_in["v"],
        "A_shift_k": _A_in["shift_k"],
        "A_ssm_k": _A_in["ssm_k"],
        "A_ssm_D": _A_in["ssm_D"],
        "B_x": _B_in["x"],
        "B_w1": _B_in["w1"],
        "B_b1": _B_in["b1"],
        "B_w2": _B_in["w2"],
        "B_b2": _B_in["b2"],
        "gate_w": _w((lambda *s: torch.randn(*s, dtype=torch.bfloat16) * 0.05)(640) * 16.0),
    }

def reference(A_q, A_k, A_v, A_shift_k, A_ssm_k, A_ssm_D, B_x, B_w1, B_b1, B_w2, B_b2, gate_w):
    def _A_ref(q, k, v, shift_k, ssm_k, ssm_D):
        B, S, D = q.shape
        ks = shift_k.shape[-1]
        k_pad = _pad(k, ((0, 0), (ks - 1, 0), (0, 0)))
        k_shift = torch.zeros_like(k)
        for t in range(ks):
            k_shift = k_shift + k_pad[:, t:t + S, :] * shift_k[:, ks - 1 - t][None, None, :]
        kv = k_shift * v
        L = 2 * S
        kv_pad = _pad(kv, ((0, 0), (0, S), (0, 0)))
        kf_pad = _pad(ssm_k, ((0, 0), (0, S)))
        kv_f = (lambda x, axes=(-2,-1): torch.fft.rfft2(x.float(), dim=axes))(kv_pad[:, None, :, :], axes=(1, 2))
        kf_f = (lambda x, axes=(-2,-1): torch.fft.rfft2(x.float(), dim=axes))(kf_pad[:, None, :], axes=(1, 2))
        kf_f = (lambda x, *d: x.permute(*d))(kf_f, 1, 2, 0)
        y_f = kv_f * kf_f[None, :, :, :]
        y = (lambda x, s, axes=(-2,-1): torch.fft.irfft2(x, s=s, dim=axes).to(torch.bfloat16))(y_f, (1, L), axes=(1, 2))[:, 0, :S, :]
        y = y + kv * ssm_D[None, None, :]
        return q * y

    def _B_ref(x, w1, b1, w2, b2):
        h = torch.einsum("bsd,df->bsf", x, w1) + b1
        h = _gelu(h)
        return torch.einsum("bsf,fd->bsd", h, w2) + b2

    x = A_q
    g = _sigmoid((x * gate_w).sum(-1, keepdims=True))
    a = _A_ref(x, A_k, A_v, A_shift_k, A_ssm_k, A_ssm_D)
    b = _B_ref(x, B_w1, B_b1, B_w2, B_b2)
    return g * a + (1 - g) * b
```

```cpp
#include <hip/hip_runtime.h>
#include <hip/hip_bf16.h>
#include <stdint.h>

typedef unsigned short u16;
__device__ __forceinline__ float bf2f(u16 b) { return __builtin_bit_cast(float, (unsigned)b << 16); }
__device__ __forceinline__ u16 f2bf(float f) { __bf16 h = (__bf16)f; return __builtin_bit_cast(u16, h); }
__device__ __forceinline__ float rbf(float f) { return bf2f(f2bf(f)); }

constexpr int S = 2048, D = 640, F = 1536;

__global__ void k_kv(const u16* k, const u16* v, const u16* sk, u16* kv) {
  int i = blockIdx.x * 256 + threadIdx.x;
  int s = i / D, d = i % D;
  float acc = 0.f;
  for (int t = 0; t < 4; ++t) {
    int ss = s + t - 3;
    float kk = ss >= 0 ? bf2f(k[ss * D + d]) : 0.f;
    acc = rbf(acc + rbf(kk * bf2f(sk[d * 4 + 3 - t])));
  }
  kv[i] = f2bf(acc * bf2f(v[i]));
}
__global__ void k_gate(const u16* q, const u16* gw, float* g) {
  int s = blockIdx.x; int l = threadIdx.x;
  float acc = 0.f;
  for (int d = l; d < D; d += 64) acc += rbf(bf2f(q[s * D + d]) * bf2f(gw[d]));
  for (int m = 32; m >= 1; m >>= 1) acc += __shfl_xor(acc, m);
  acc = rbf(acc);
  if (l == 0) g[s] = rbf(1.f / (1.f + __expf(-acc)));
}
__global__ void k_conv(const u16* kv, const u16* f, const u16* Dv, const u16* q, u16* a) {
  int i = blockIdx.x * 256 + threadIdx.x;
  int t = i / D, d = i % D;
  float acc = 0.f;
  for (int s = 0; s <= t; ++s) acc += bf2f(kv[s * D + d]) * bf2f(f[d * S + t - s]);
  float y = rbf(acc);
  y = rbf(y + rbf(bf2f(kv[i]) * bf2f(Dv[d])));
  a[i] = f2bf(bf2f(q[i]) * y);
}
__global__ void k_g1(const u16* x, const u16* w1, const u16* b1, u16* h) {
  int i = blockIdx.x * 256 + threadIdx.x;
  int s = i / F, f = i % F;
  float acc = 0.f;
  for (int d = 0; d < D; ++d) acc += bf2f(x[s * D + d]) * bf2f(w1[d * F + f]);
  float z = rbf(rbf(acc) + bf2f(b1[f]));
  float u = 0.7978845608028654f * (z + 0.044715f * z * z * z);
  float th = tanhf(u);
  h[i] = f2bf(0.5f * z * (1.f + th));
}
__global__ void k_g2(const u16* h, const u16* w2, const u16* b2, const u16* a, const float* g, u16* out) {
  int i = blockIdx.x * 256 + threadIdx.x;
  int s = i / D, d = i % D;
  float acc = 0.f;
  for (int f = 0; f < F; ++f) acc += bf2f(h[s * F + f]) * bf2f(w2[f * D + d]);
  float b = rbf(rbf(acc) + bf2f(b2[d]));
  float gg = g[s];
  out[i] = f2bf(rbf(gg * bf2f(a[i])) + rbf(rbf(1.f - gg) * b));
}

extern "C" void kernel_launch(void* const* d_in, const int* in_sizes, int n_in,
                              void* d_out, int out_size, void* d_ws, size_t ws_size,
                              hipStream_t stream) {
  const u16* q = (const u16*)d_in[0]; const u16* k = (const u16*)d_in[1]; const u16* v = (const u16*)d_in[2];
  const u16* sk = (const u16*)d_in[3]; const u16* ssmk = (const u16*)d_in[4]; const u16* ssmD = (const u16*)d_in[5];
  const u16* w1 = (const u16*)d_in[7]; const u16* b1 = (const u16*)d_in[8]; const u16* w2 = (const u16*)d_in[9];
  const u16* b2 = (const u16*)d_in[10]; const u16* gw = (const u16*)d_in[11];
  char* ws = (char*)d_ws;
  u16* kv = (u16*)ws; u16* a = (u16*)(ws + (4 << 20)); u16* h = (u16*)(ws + (8 << 20)); float* g = (float*)(ws + (16 << 20));
  k_kv<<<S * D / 256, 256, 0, stream>>>(k, v, sk, kv);
  k_gate<<<S, 64, 0, stream>>>(q, gw, g);
  k_conv<<<S * D / 256, 256, 0, stream>>>(kv, ssmk, ssmD, q, a);
  k_g1<<<S * F / 256, 256, 0, stream>>>(q, w1, b1, h);
  k_g2<<<S * D / 256, 256, 0, stream>>>(h, w2, b2, a, g, (u16*)d_out);
}
```

```cpp
#include <hip/hip_runtime.h>
#include <stdint.h>

typedef unsigned short u16;
typedef short s16x8 __attribute__((ext_vector_type(8)));
typedef short s16x4 __attribute__((ext_vector_type(4)));
typedef float f32x4 __attribute__((ext_vector_type(4)));
typedef float f32x2 __attribute__((ext_vector_type(2)));
typedef __bf16 bf16x2 __attribute__((ext_vector_type(2)));
typedef unsigned u32x4 __attribute__((ext_vector_type(4)));
typedef unsigned u32x2 __attribute__((ext_vector_type(2)));
typedef int i32x4 __attribute__((ext_vector_type(4)));
#define LAS __attribute__((address_space(3)))
#define DEV __device__ __forceinline__
typedef __amdgpu_buffer_rsrc_t rsrc_t;

constexpr int S = 2048, D = 640, F = 1536;
constexpr unsigned RW_PITCH = 4096 + 128;
constexpr unsigned OFF_E = 0, OFF_F0 = 4096, OFF_F1 = 8192, OFF_F2 = 12288;
constexpr unsigned OFF_RW = 1u << 20, OFF_W2T = 4u << 20, OFF_H = 6u << 20, OFF_Z = 13u << 20, OFF_G = 16u << 20, WS_USED = 17u << 20;
constexpr int LDS_BYTES = 160 * 1024;
constexpr unsigned L_CONV = 98304, CONV_CH = 17600, L_XCH = 133504, L_TO = 149888;
constexpr int AUX_SC1 = 16;
constexpr unsigned SPIN_CAP = 1u << 16;

struct Params {
  const u16 *q, *k, *v, *sk, *fk, *ssd, *w1, *b1, *w2, *b2, *gw;
  u16* out;
  char* ws;
};

DEV f32x4 mfma(u32x4 a, u32x4 b, f32x4 c) { return __builtin_amdgcn_mfma_f32_16x16x32_bf16(__builtin_bit_cast(s16x8, a), __builtin_bit_cast(s16x8, b), c, 0, 0, 0); }
DEV i32x4 mksrd(const void* p, unsigned bytes) {
  unsigned long long a = (unsigned long long)p;
  i32x4 r; r.x = (int)(unsigned)a; r.y = (int)(unsigned)(a >> 32); r.z = (int)bytes; r.w = 0x00020000; return r;
}
DEV rsrc_t mkrsrc(const void* p, unsigned bytes) { return __builtin_amdgcn_make_buffer_rsrc((void*)p, 0, (int)bytes, 0x00020000); }
template <int POL, int IMM> DEV void dma16(i32x4 srd, unsigned voff, unsigned soff, unsigned lds) {
  if (POL == 0) asm volatile("s_mov_b32 m0, %2\n\ts_nop 0\n\tbuffer_load_dwordx4 %0, %1, %3 offen offset:%4 lds" :: "v"(voff), "s"(srd), "s"(lds), "s"(soff), "i"(IMM) : "memory");
  if (POL == 1) asm volatile("s_mov_b32 m0, %2\n\ts_nop 0\n\tbuffer_load_dwordx4 %0, %1, %3 offen offset:%4 nt lds" :: "v"(voff), "s"(srd), "s"(lds), "s"(soff), "i"(IMM) : "memory");
  if (POL == 2) asm volatile("s_mov_b32 m0, %2\n\ts_nop 0\n\tbuffer_load_dwordx4 %0, %1, %3 offen offset:%4 sc1 lds" :: "v"(voff), "s"(srd), "s"(lds), "s"(soff), "i"(IMM) : "memory");
}
template <int N> DEV void waitvm() { asm volatile("s_waitcnt vmcnt(%0)" :: "i"(N) : "memory"); }
DEV void waitlgkm0() { asm volatile("s_waitcnt lgkmcnt(0)" ::: "memory"); }
DEV void bar() { asm volatile("s_barrier" ::: "memory"); }
DEV void sched0() { __builtin_amdgcn_sched_barrier(0); }
DEV unsigned opq(unsigned x) { asm volatile("" : "+v"(x)); return x; }
DEV u32x4 lds128(unsigned a) { return *(LAS u32x4*)a; }
DEV u32x2 lds64(unsigned a) { return *(LAS u32x2*)a; }
DEV u32x2 ldstr(unsigned a) { return __builtin_bit_cast(u32x2, __builtin_amdgcn_ds_read_tr16_b64_v4i16((LAS s16x4*)a)); }
DEV unsigned pk2(float lo, float hi) { f32x2 z = {lo, hi}; return __builtin_bit_cast(unsigned, __builtin_convertvector(z, bf16x2)); }
DEV float blo(unsigned w) { return __builtin_bit_cast(float, w << 16); }
DEV float bhi(unsigned w) { return __builtin_bit_cast(float, w & 0xffff0000u); }
DEV float dot2(unsigned a, unsigned b, float c) { return __builtin_amdgcn_fdot2_f32_bf16(__builtin_bit_cast(bf16x2, a), __builtin_bit_cast(bf16x2, b), c, false); }

#define TS(idx) do {} while (0)
#define TSDECL
#define TSARG
#define TSPASS
#define TSFLUSH(lo, hi)

DEV u32x2 flag_load(rsrc_t rws, unsigned voff) { return __builtin_amdgcn_raw_buffer_load_b64(rws, voff, 0, AUX_SC1); }
DEV void flag_wait(rsrc_t rws, unsigned voff, u32x2 first, u32x2 tag) {
  if (__builtin_expect(__builtin_amdgcn_ballot_w64(first.x != tag.x || first.y != tag.y) != 0ull, 0)) {
    unsigned spins = 0;
    unsigned long long bad;
#pragma unroll 1
    do {
      __builtin_amdgcn_s_sleep(1);
      asm volatile("" ::: "memory");
      const u32x2 v = flag_load(rws, voff);
      bad = __builtin_amdgcn_ballot_w64(v.x != tag.x || v.y != tag.y);
    } while (bad != 0ull && ++spins < SPIN_CAP);
  }
  asm volatile("" ::: "memory");
}
DEV void flag_store(rsrc_t rws, unsigned off, u32x2 tag) {
  __builtin_amdgcn_raw_buffer_store_b64(tag, rws, threadIdx.x == 0 ? 0u : 0x80000000u, off, AUX_SC1);
}

DEV void phase_p0(const Params& p, rsrc_t rws, int hz, int w, int lane) {
  const i32x4 sk_ = mksrd(p.k, S * D * 2), sv_ = mksrd(p.v, S * D * 2), sw2 = mksrd(p.w2, F * D * 2);
  const int rt = hz >> 1, hf = hz & 1, s0 = rt * 64;
  const int g = lane >> 4, q4 = (lane >> 2) & 3, p4 = lane & 3, i16 = lane & 15;
  constexpr unsigned SLAB = 17408, L_W2 = 87040;
  u32x2 tap[5];
#pragma unroll
  for (int j = 0; j < 5; ++j) asm volatile("global_load_dwordx2 %0, %1, off" : "=v"(tap[j]) : "v"(p.sk + (hf * 320 + j * 64 + w * 16 + i16) * 4) : "memory");
  const unsigned vrow = (lane >> 3) * 1280 + (lane & 7) * 16 + hf * 640;
  dma16<1, 0>(sk_, vrow + (unsigned)((s0 - 8) * 1280) + w * 128, 0, w * SLAB);
  dma16<1, 0>(sk_, vrow + (unsigned)((s0 - 8) * 1280) + 4 * 128, 0, 4 * SLAB);
#pragma unroll
  for (int j = 0; j < 5; ++j) {
#pragma unroll
    for (int n = 0; n < 2; ++n) dma16<1, 0>(sk_, vrow, (s0 + w * 16 + n * 8) * 1280 + j * 128, j * SLAB + 1024 + (w * 2 + n) * 1024);
#pragma unroll
    for (int n = 0; n < 2; ++n) dma16<1, 0>(sv_, vrow, (s0 + w * 16 + n * 8) * 1280 + j * 128, j * SLAB + 9216 + (w * 2 + n) * 1024);
  }
#pragma unroll
  for (int n = 0; n < 10; ++n) dma16<1, 0>(sw2, hz < 48 ? lane * 16 + hz * 40960 : 0x80000000u, (w * 10 + n) * 1024, L_W2 + (w * 10 + n) * 1024);
#pragma unroll
  for (int j = 0; j < 5; ++j) {
    if (j == 0) waitvm<26>(); else if (j == 1) waitvm<25>(); else if (j == 2) waitvm<24>(); else if (j == 3) waitvm<23>(); else waitvm<22>();
    bar();
    asm volatile("" : "+v"(tap[j]));
    const unsigned ka = j * SLAB + (4 + 16 * g + q4) * 128 + w * 32 + p4 * 8;
    const unsigned va = j * SLAB + 9216 + (16 * g + q4) * 128 + w * 32 + p4 * 8;
    unsigned K[10], V[8];
#pragma unroll
    for (int n = 0; n < 5; ++n) { const u32x2 t = ldstr(ka + n * 512); K[2 * n] = t.x; K[2 * n + 1] = t.y; }
#pragma unroll
    for (int n = 0; n < 4; ++n) { const u32x2 t = ldstr(va + n * 512); V[2 * n] = t.x; V[2 * n + 1] = t.y; }
    const unsigned w01 = tap[j].x, w23 = tap[j].y;
    const unsigned W32 = __builtin_amdgcn_alignbit(w23, w23, 16), W10 = __builtin_amdgcn_alignbit(w01, w01, 16);
    const unsigned Wz3 = w23 & 0xffff0000u, W21 = (w23 & 0xffffu) | (w01 & 0xffff0000u), W0z = w01 & 0xffffu;
    float o[16];
#pragma unroll
    for (int a = 0; a < 8; ++a) {
      const float ev = dot2(K[a + 2], W0z, dot2(K[a + 1], W21, dot2(K[a], Wz3, 0.f)));
      const float od = dot2(K[a + 2], W10, dot2(K[a + 1], W32, 0.f));
      o[2 * a] = ev * blo(V[a]); o[2 * a + 1] = od * bhi(V[a]);
    }
    u32x4 r0, r1;
    r0.x = pk2(o[15], o[14]); r0.y = pk2(o[13], o[12]); r0.z = pk2(o[11], o[10]); r0.w = pk2(o[9], o[8]);
    r1.x = pk2(o[7], o[6]); r1.y = pk2(o[5], o[4]); r1.z = pk2(o[3], o[2]); r1.w = pk2(o[1], o[0]);
    const unsigned ta = L_TO + w * 2304 + i16 * 144 + (96 - 32 * g);
    *(LAS u32x4*)ta = r0; *(LAS u32x4*)(ta + 16) = r1;
    const unsigned ra = L_TO + w * 2304 + (lane >> 3) * 144 + (lane & 7) * 16;
    const u32x4 t0 = lds128(ra), t1 = lds128(ra + 8 * 144);
    const unsigned ch0 = hf * 320 + j * 64 + w * 16;
    const unsigned vo = (lane >> 3) * RW_PITCH + (lane & 7) * 16;
    const unsigned so = OFF_RW + ch0 * RW_PITCH + (1984 - s0) * 2;
    __builtin_amdgcn_raw_buffer_store_b128(t0, rws, vo, so, AUX_SC1);
    __builtin_amdgcn_raw_buffer_store_b128(t1, rws, vo, so + 8 * RW_PITCH, AUX_SC1);
    __builtin_amdgcn_raw_buffer_store_b128(u32x4{0u, 0u, 0u, 0u}, rws, (rt == 0 && lane < 16) ? lane * RW_PITCH + 4096 : 0x80000000u, OFF_RW + ch0 * RW_PITCH, AUX_SC1);
    sched0();
  }
  waitvm<15>(); bar();
  if (hz < 48) {
#pragma unroll
    for (int n = 0; n < 10; ++n) {
      const int cb = w * 2 + n / 5, T = n % 5;
      const unsigned col = cb * 80 + (T < 4 ? 32 * (T >> 1) + 8 * p4 + 4 * (T & 1) : 64 + 4 * p4);
      const unsigned a = L_W2 + (4 * g + q4) * 1280 + col * 2;
      const u32x2 lo = ldstr(a), hi = ldstr(a + 16 * 1280);
      const u32x4 o = {lo.x, lo.y, hi.x, hi.y};
      __builtin_amdgcn_raw_buffer_store_b128(o, rws, lane * 16, OFF_W2T + ((cb * 48 + hz) * 5 + T) * 1024, AUX_SC1);
    }
  }
}

DEV void phase_gate(const Params& p, rsrc_t rws, int row0, int w, int lane) {
  const int g = lane >> 4, i16 = lane & 15;
  u32x4 gw[5], x[2][5];
#pragma unroll
  for (int n = 0; n < 5; ++n) gw[n] = *(const u32x4*)(p.gw + (n * 16 + i16) * 8);
#pragma unroll
  for (int r = 0; r < 2; ++r)
#pragma unroll
    for (int n = 0; n < 5; ++n) x[r][n] = *(const u32x4*)(p.q + (row0 + w * 8 + r * 4 + g) * D + (n * 16 + i16) * 8);
#pragma unroll
  for (int r = 0; r < 2; ++r) {
    float a = 0.f;
#pragma unroll
    for (int n = 0; n < 5; ++n) { a = dot2(x[r][n].x, gw[n].x, a); a = dot2(x[r][n].y, gw[n].y, a); a = dot2(x[r][n].z, gw[n].z, a); a = dot2(x[r][n].w, gw[n].w, a); }
    a += __shfl_xor(a, 1); a += __shfl_xor(a, 2); a += __shfl_xor(a, 4); a += __shfl_xor(a, 8);
    const float gg = __builtin_amdgcn_rcpf(1.f + __builtin_amdgcn_exp2f(-1.4426950408889634f * a));
    __builtin_amdgcn_raw_buffer_store_b32(__builtin_bit_cast(unsigned, gg), rws, i16 == 0 ? (unsigned)(row0 + w * 8 + r * 4 + g) * 4 : 0x80000000u, OFF_G, AUX_SC1);
  }
}

DEV void conv_pads(unsigned lbase, int w, int lane) {
  const unsigned cbase = lbase + (w >> 1) * CONV_CH, half = w & 1;
  const unsigned rb = cbase + half * 4576, fb = cbase + 9152 + half * 4224;
  const int l = lane & 31;
  *(LAS u32x4*)(l < 28 ? rb + 4096 + l * 16 : fb + (l - 28) * 16) = u32x4{0u, 0u, 0u, 0u};
}
template <int N> DEV void conv_load_f(const Params& p, int c0, unsigned lbase, int w, int lane) {
  const int ch = c0 + (w >> 1), half = w & 1;
  dma16<1, N * 1024>(mksrd(p.fk, D * S * 2), lane * 16, ch * 4096, lbase + (w >> 1) * CONV_CH + 9152 + half * 4224 + 60 + half * 4);
}
DEV void conv_load_r(const Params& p, int c0, unsigned lbase, int w, int lane) {
  const int ch = c0 + (w >> 1), half = w & 1;
  const i32x4 sr = mksrd(p.ws, WS_USED);
  const unsigned so = OFF_RW + ch * RW_PITCH + 2 - 2 * half, lb = lbase + (w >> 1) * CONV_CH + half * 4576;
  dma16<2, 0>(sr, lane * 16, so, lb); dma16<2, 1024>(sr, lane * 16, so, lb); dma16<2, 2048>(sr, lane * 16, so, lb); dma16<2, 3072>(sr, lane * 16, so, lb);
}

struct G2Ctx { unsigned wbase, hbase; int rot, own; };
DEV u32x4 load_w(rsrc_t rws, const G2Ctx& c, int lane, int n) {
  int ks = n / 5 + c.rot; if (ks >= 12) ks -= 12;
  return __builtin_amdgcn_raw_buffer_load_b128(rws, lane * 16 + ((n % 5) & 3) * 1024, c.wbase + ks * 5120 + ((n % 5) >> 2) * 4096, AUX_SC1);
}
DEV u32x4 load_h(rsrc_t rws, const G2Ctx& c, int lane, int n) {
  int ks = n % 12 + c.rot; if (ks >= 12) ks -= 12;
  return __builtin_amdgcn_raw_buffer_load_b128(rws, lane * 16, c.hbase + (((c.own + 1 + n / 12) & 3) * 48 + ks) * 1024, AUX_SC1);
}

template <int W0, int NW, bool PUB>
DEV void conv_compute(const Params& p, rsrc_t rws, int c0, unsigned lbase, unsigned dbits_in, int w, int lane, const G2Ctx& c2, u32x4 (&W)[60],
                      u32x2 tag, unsigned pub_off, unsigned poll_off, u32x2& poll, f32x4 (&acc)[8]) {
  const int half = w & 1, g = lane >> 4, n16 = lane & 15;
  const unsigned cbase = lbase + (w >> 1) * CONV_CH;
  const unsigned ua = opq(cbase + (n16 & 1) * 4576 + 448 - 64 * (n16 >> 1) + 16 * g + 256 * half);
  const unsigned fa = opq(cbase + 9152 + (n16 & 1) * 4228 + 4 * n16 + 16 * g + 256 * half);
  const unsigned fa2 = opq(fa + 8);
  const unsigned dbits = half ? 0u : dbits_in;
  const int dsel = 15 - n16 - 4 * g;
  const u32x4 Df = {dsel == 0 ? dbits : 0u, dsel == 1 ? dbits : 0u, dsel == 2 ? dbits : 0u, dsel == 3 ? dbits : 0u};
  u32x4 U[2][8]; u32x2 Fl[2][8], Fh[2][8];
#pragma unroll
  for (int j = 0; j < 8; ++j) { U[0][j] = lds128(ua + 512 * (7 - j)); sched0(); }
#pragma unroll
  for (int j = 0; j < 8; ++j) { Fl[0][j] = lds64(fa + 512 * j); Fh[0][j] = lds64(fa2 + 512 * j); sched0(); }
#pragma unroll
  for (int I = 0; I < 8; ++I) { acc[I] = mfma(U[0][I], Df, f32x4{0.f, 0.f, 0.f, 0.f}); sched0(); }
  int wn = 0;
#pragma unroll
  for (int r = 0; r < 4; ++r) {
    const int cur = r & 1, nxt = cur ^ 1;
    int m = 0;
#pragma unroll
    for (int E = 0; E < 8; ++E)
#pragma unroll
      for (int I = E; I < 8; ++I) {
        acc[I] = mfma(U[cur][I - E], u32x4{Fl[cur][E].x, Fl[cur][E].y, Fh[cur][E].x, Fh[cur][E].y}, acc[I]);
        sched0();
        if (r < 3 && m < 32 && (m & 3) != 3) {
          const int x = (m >> 2) * 3 + (m & 3);
          if (x < 8) U[nxt][x] = lds128(ua + 512 * (7 - x) + 64 * (r + 1));
          else if (x & 1) Fh[nxt][(x - 8) >> 1] = lds64(fa2 + 512 * ((x - 8) >> 1) + 64 * (r + 1));
          else Fl[nxt][(x - 8) >> 1] = lds64(fa + 512 * ((x - 8) >> 1) + 64 * (r + 1));
          sched0();
        }
        if ((m & 3) == 3 && m < 32 && wn < NW) { W[W0 + wn] = load_w(rws, c2, lane, W0 + wn); ++wn; sched0(); }
        ++m;
      }
    if (PUB && r == 1) { waitvm<16>(); bar(); flag_store(rws, pub_off, tag); sched0(); }
    if (PUB && r == 2) { poll = flag_load(rws, poll_off); sched0(); }
  }
}
DEV void conv_finish(rsrc_t rws, int c0, int w, int lane, f32x4 (&acc)[8]) {
  const int ch = c0 + (w >> 1), half = w & 1;
  const unsigned xw = L_XCH + w * 4096 + lane * 16, xr = L_XCH + (w ^ 1) * 4096 + lane * 16;
  if (half == 0) {
#pragma unroll
    for (int i = 0; i < 4; ++i) *(LAS f32x4*)(xw + i * 1024) = acc[4 + i];
  } else {
#pragma unroll
    for (int i = 0; i < 4; ++i) { *(LAS f32x4*)(xw + i * 1024) = acc[i]; acc[i] = acc[4 + i]; }
  }
  waitlgkm0(); bar();
#pragma unroll
  for (int i = 0; i < 4; ++i) {
    const f32x4 y = acc[i] + *(LAS f32x4*)(xr + i * 1024);
    const u32x2 o = {pk2(y[0], y[1]), pk2(y[2], y[3])};
    __builtin_amdgcn_raw_buffer_store_b64(o, rws, lane * 8, OFF_Z + ch * 4096 + (half * 4 + i) * 512, AUX_SC1);
  }
  sched0();
}

DEV f32x2 gelu2(f32x2 z) {
  const float c0 = -2.f * 0.7978845608028654f * 1.4426950408889634f, c1 = c0 * 0.044715f;
  const f32x2 a = z * __builtin_elementwise_fma(z * z, f32x2{c1, c1}, f32x2{c0, c0});
  const f32x2 d = f32x2{__builtin_amdgcn_exp2f(a.x), __builtin_amdgcn_exp2f(a.y)} + f32x2{1.f, 1.f};
  return z * f32x2{__builtin_amdgcn_rcpf(d.x), __builtin_amdgcn_rcpf(d.y)};
}
DEV unsigned gelu_pk(float a, float b) { const f32x2 r = gelu2(f32x2{a, b}); return pk2(r.x, r.y); }

DEV void phase_g1(const Params& p, rsrc_t rws, int rt, int ct, int k0, int c0, int w, int lane, const G2Ctx& c2, u32x4 (&W)[60], u32x2& tag TSARG) {
  const i32x4 sx = mksrd(p.q, S * D * 2), sw = mksrd(p.w1, D * F * 2);
  const int g = lane >> 4, q4 = (lane >> 2) & 3, p4 = lane & 3, n16 = lane & 15, wf = w & 1, wsn = w >> 1;
  unsigned vx[4], vw[4];
#pragma unroll
  for (int j = 0; j < 4; ++j) {
    vx[j] = (((rt * 128 + w * 32 + (lane >> 3)) * 1280 + ((lane & 7) ^ g) * 16) ^ ((j & 1) * 64)) + j * (8 * 1280 - 1024);
    vw[j] = (((w * 16 + g) * 3072 + ct * 256 + (n16 ^ (g << 1)) * 16) ^ ((j >> 1) * 128)) + j * (4 * 3072 - 1024);
  }
  auto issue = [&](int st, int j) {
    int ks = st + k0; if (ks >= 10) ks -= 10;
    const unsigned base = (st % 3) * 32768 + w * 4096;
    if (j == 0) dma16<0, 0>(sx, vx[0], ks * 128, base);
    if (j == 1) dma16<0, 1024>(sx, vx[1], ks * 128, base);
    if (j == 2) dma16<0, 2048>(sx, vx[2], ks * 128, base);
    if (j == 3) dma16<0, 3072>(sx, vx[3], ks * 128, base);
    if (j == 4) dma16<0, 0>(sw, vw[0], ks * 64 * 3072, base + 16384);
    if (j == 5) dma16<0, 1024>(sw, vw[1], ks * 64 * 3072, base + 16384);
    if (j == 6) dma16<0, 2048>(sw, vw[2], ks * 64 * 3072, base + 16384);
    if (j == 7) dma16<0, 3072>(sw, vw[3], ks * 64 * 3072, base + 16384);
  };
  u32x2 bias[4];
#pragma unroll
  for (int a = 0; a < 4; ++a) asm volatile("global_load_dwordx2 %0, %1, off" : "=v"(bias[a]) : "v"(p.b1 + ct * 128 + wf * 64 + a * 16 + g * 4) : "memory");
#pragma unroll
  for (int st = 0; st < 3; ++st)
#pragma unroll
    for (int j = 0; j < 8; ++j) issue(st, j);
  conv_pads(L_CONV, w, lane);
  unsigned wa[2][4], xa[2][2];
#pragma unroll
  for (int a = 0; a < 4; ++a) {
    wa[0][a] = opq(16384 + (8 * g + q4) * 256 + p4 * 8 + ((((wf ^ (g & 1)) << 2) | (a ^ q4)) * 32));
    wa[1][a] = opq(wa[0][a] + 65536);
  }
  xa[0][0] = opq((wsn * 64 + n16) * 128 + ((g ^ (n16 >> 1)) * 16)); xa[0][1] = opq(xa[0][0] ^ 64);
  xa[1][0] = opq(xa[0][0] + 65536); xa[1][1] = opq(xa[0][1] + 65536);
  f32x4 acc[4][4];
  u32x2 Al[2][4], Ah[2][4]; u32x4 B[2][4];
  auto rd = [&](int buf, int st, int kk, int x) {
    const int sl = st % 3, hi = sl == 2;
    const unsigned base = (sl & 1) * 32768;
    if (x < 4) B[buf][x] = lds128(xa[hi][kk] + base + x * 2048);
    else { Al[buf][x - 4] = ldstr(wa[hi][x - 4] + base + kk * 8192); Ah[buf][x - 4] = ldstr(wa[hi][x - 4] + base + kk * 8192 + 1024); }
  };
  waitvm<16>(); bar();
  TS(1);
#pragma unroll
  for (int x = 0; x < 8; ++x) rd(0, 0, 0, x);
#pragma unroll
  for (int a = 0; a < 4; ++a) {
    asm volatile("" : "+v"(bias[a]));
    const f32x4 bv = {blo(bias[a].x), bhi(bias[a].x), blo(bias[a].y), bhi(bias[a].y)};
#pragma unroll
    for (int b = 0; b < 4; ++b) acc[a][b] = bv;
  }
  sched0();
  u32x2 poll;
#pragma unroll
  for (int st = 0; st < 10; ++st) {
#pragma unroll
    for (int m = 0; m < 16; ++m) {
      const int a = m >> 2, b = m & 3;
      acc[a][b] = mfma(u32x4{Al[0][a].x, Al[0][a].y, Ah[0][a].x, Ah[0][a].y}, B[0][b], acc[a][b]);
      sched0();
      if (m < 8) { rd(1, st, 1, m); sched0(); }
      else if (m & 1) { if (st >= 1 && st + 2 < 10) { issue(st + 2, 4 + ((m - 8) >> 1)); sched0(); } }
    }
    if (st < 9) {
      waitlgkm0();
      if (st < 8) waitvm<8>(); else waitvm<4>();
      bar();
    }
#pragma unroll
    for (int m = 0; m < 16; ++m) {
      const int a = m >> 2, b = m & 3;
      acc[a][b] = mfma(u32x4{Al[1][a].x, Al[1][a].y, Ah[1][a].x, Ah[1][a].y}, B[1][b], acc[a][b]);
      sched0();
      if (m < 8) { if (st < 9) { rd(0, st + 1, 0, m); sched0(); } }
      else if (m & 1) {
        if (st + 3 < 10) { issue(st + 3, (m - 8) >> 1); sched0(); }
        if (st == 7) {
          if (m == 9) conv_load_f<0>(p, c0, L_CONV, w, lane);
          if (m == 11) conv_load_f<1>(p, c0, L_CONV, w, lane);
          if (m == 13) conv_load_f<2>(p, c0, L_CONV, w, lane);
          if (m == 15) conv_load_f<3>(p, c0, L_CONV, w, lane);
          sched0();
        }
        if (st == 8 && m == 15) { poll = flag_load(rws, OFF_F0 + lane * 8); sched0(); }
      }
    }
  }
  TS(2);
  tag.x = opq(tag.x) + 1u;
  flag_wait(rws, OFF_F0 + lane * 8, poll, tag);
  TS(3);
  conv_load_r(p, c0, L_CONV, w, lane);
  sched0();
#pragma unroll
  for (int n = 0; n < 8; ++n) {
    const int b = n >> 1, P = n & 1;
    const f32x4 u = acc[2 * P][b], v = acc[2 * P + 1][b];
    u32x4 o;
    if (n * 4 + 0 < 28) { W[n * 4 + 0] = load_w(rws, c2, lane, n * 4 + 0); sched0(); }
    o.x = gelu_pk(u[0], u[1]); sched0();
    if (n * 4 + 1 < 28) { W[n * 4 + 1] = load_w(rws, c2, lane, n * 4 + 1); sched0(); }
    o.y = gelu_pk(u[2], u[3]); sched0();
    if (n * 4 + 2 < 28) { W[n * 4 + 2] = load_w(rws, c2, lane, n * 4 + 2); sched0(); }
    o.z = gelu_pk(v[0], v[1]); sched0();
    if (n * 4 + 3 < 28) { W[n * 4 + 3] = load_w(rws, c2, lane, n * 4 + 3); sched0(); }
    o.w = gelu_pk(v[2], v[3]);
    __builtin_amdgcn_raw_buffer_store_b128(o, rws, lane * 16, OFF_H + ((rt * 8 + wsn * 4 + b) * 48 + ct * 4 + wf * 2 + P) * 1024, AUX_SC1);
    sched0();
  }
  waitvm<36>(); bar();
}

__global__ __launch_bounds__(256, 1) void k_fused(Params p) {
  extern __shared__ __attribute__((aligned(16))) char smem[];
  const int b = blockIdx.x, xcd = b & 7, i = b >> 3;
  const int w = __builtin_amdgcn_readfirstlane(threadIdx.x >> 6), lane = threadIdx.x & 63;
  TSDECL; TS(0);
  const rsrc_t rws = mkrsrc(p.ws, WS_USED), rsd = mkrsrc(p.ssd, D * 2);
  u32x2 tag;
  const int ri = i >> 3, cb = i & 7, R = xcd * 4 + ri;
  G2Ctx c2;
  c2.rot = (3 * ri + 6 * (cb >> 2)) % 12; c2.own = (w + cb) & 3;
  c2.wbase = OFF_W2T + (cb * 48 + w * 12) * 5120; c2.hbase = OFF_H + (R * 4 * 48 + w * 12) * 1024;
  u32x4 W[60];
  f32x4 cacc[8];
  u32x2 poll;
  int c0; unsigned pub_off, dbits;
  if (i >= 24) {
    const int hz = (i - 24) * 8 + xcd;
    const int cA = (192 + 2 * hz) * 2;
    c0 = cA + 2;
    tag = flag_load(rws, OFF_E);
    const unsigned dA = __builtin_amdgcn_raw_buffer_load_b16(rsd, 0, (cA + (w >> 1)) * 2, 0);
    dbits = __builtin_amdgcn_raw_buffer_load_b16(rsd, 0, (c0 + (w >> 1)) * 2, 0);
    sched0();
    phase_p0(p, rws, hz, w, lane);
    TS(1);
    waitvm<0>(); waitlgkm0(); bar();
    tag.x = opq(tag.x) + 1u;
    flag_store(rws, OFF_F0 + hz * 8, tag);
    TS(2);
    conv_pads(0, w, lane); conv_pads(L_CONV, w, lane);
    waitlgkm0();
    conv_load_f<0>(p, cA, 0, w, lane); conv_load_f<1>(p, cA, 0, w, lane); conv_load_f<2>(p, cA, 0, w, lane); conv_load_f<3>(p, cA, 0, w, lane);
    conv_load_f<0>(p, c0, L_CONV, w, lane); conv_load_f<1>(p, c0, L_CONV, w, lane); conv_load_f<2>(p, c0, L_CONV, w, lane); conv_load_f<3>(p, c0, L_CONV, w, lane);
    phase_gate(p, rws, xcd * 256 + (i - 24) * 32, w, lane);
    sched0();
    flag_wait(rws, OFF_F0 + lane * 8, flag_load(rws, OFF_F0 + lane * 8), tag);
    TS(3);
    conv_load_r(p, cA, 0, w, lane); conv_load_r(p, c0, L_CONV, w, lane);
    waitvm<4>(); bar();
    conv_compute<0, 28, false>(p, rws, cA, 0, dA, w, lane, c2, W, tag, 0, 0, poll, cacc);
    conv_finish(rws, cA, w, lane, cacc);
    waitvm<32>(); bar();
    pub_off = OFF_F2 + (192 + 2 * hz) * 8;
  } else {
    const int r = i / 6, c = i % 6, gi = i * 8 + xcd;
    const int rt = (xcd >> 1) * 4 + r, ct = (xcd & 1) * 6 + c;
    c0 = gi * 2;
    tag = flag_load(rws, OFF_E);
    dbits = __builtin_amdgcn_raw_buffer_load_b16(rsd, 0, (c0 + (w >> 1)) * 2, 0);
    sched0();
    phase_g1(p, rws, rt, ct, (((2 * c + 3 * r) % 12) * 10 / 12 + xcd) % 10, c0, w, lane, c2, W, tag TSPASS);
    pub_off = OFF_F1 + (rt * 12 + ct) * 8;
  }
  TS(4);
  const unsigned f1_off = OFF_F1 + ((R >> 1) * 12 + (lane < 12 ? lane : 0)) * 8;
  conv_compute<28, 32, true>(p, rws, c0, L_CONV, dbits, w, lane, c2, W, tag, pub_off, f1_off, poll, cacc);
  TS(5);
  flag_wait(rws, f1_off, poll, tag);
  TS(6);
  u32x4 H[48];
  constexpr int LA = 12;
#pragma unroll
  for (int n = 0; n < LA; ++n) H[n] = load_h(rws, c2, lane, n);
  sched0();
  conv_finish(rws, c0, w, lane, cacc);
  TS(7);
  const int g = lane >> 4, c16 = lane & 15, own = c2.own;
  const int s = R * 64 + own * 16 + c16, d0 = cb * 80;
  const int hzA = (2 * ri) * 8 + xcd;
  const unsigned f2_off = OFF_F2 + (lane < 40 ? cb * 40 + lane : 192 + 2 * (hzA + (lane & 1) * 8)) * 8;
  f32x4 acc[5];
  u32x4 q0, q1, e0, e1; u32x2 q2, e2; unsigned Z[20]; float gg;
#pragma unroll
  for (int pp = 0; pp < 4; ++pp) {
#pragma unroll
    for (int ii = 0; ii < 12; ++ii) {
      const int n = pp * 12 + ii;
#pragma unroll
      for (int T = 0; T < 5; ++T) acc[T] = mfma(W[ii * 5 + T], H[n], ii == 0 ? f32x4{0.f, 0.f, 0.f, 0.f} : acc[T]);
      sched0();
      if (n + LA < 48) { H[n + LA] = load_h(rws, c2, lane, n + LA); sched0(); }
      if (n == 7) { waitvm<8>(); bar(); flag_store(rws, OFF_F2 + (c0 >> 1) * 8, tag); sched0(); }
      if (n == 30) { poll = flag_load(rws, f2_off); sched0(); }
    }
    if (pp < 3) {
#pragma unroll
      for (int T = 0; T < 5; ++T) *(LAS f32x4*)((((own + 1 + pp) & 3) * 3 + pp) * 5120 + T * 1024 + lane * 16) = acc[T];
      sched0();
    }
    if (pp == 2) {
      TS(8);
      q0 = *(const u32x4*)(p.q + s * D + d0 + 8 * g); q1 = *(const u32x4*)(p.q + s * D + d0 + 32 + 8 * g);
      q2 = *(const u32x2*)(p.q + s * D + d0 + 64 + 4 * g);
      e0 = *(const u32x4*)(p.b2 + d0 + 8 * g); e1 = *(const u32x4*)(p.b2 + d0 + 32 + 8 * g);
      e2 = *(const u32x2*)(p.b2 + d0 + 64 + 4 * g);
      flag_wait(rws, f2_off, poll, tag);
      TS(9);
      gg = __builtin_bit_cast(float, __builtin_amdgcn_raw_buffer_load_b32(rws, s * 4, OFF_G, AUX_SC1));
      const unsigned zo = OFF_Z + d0 * 4096 + R * 128 + (own >> 1) * 4 + (own & 1) * 64;
      const unsigned zv8 = g * 8 * 4096 + (c16 >> 1) * 8, zv4 = g * 4 * 4096 + (c16 >> 1) * 8;
#pragma unroll
      for (int j = 0; j < 16; ++j) Z[j] = __builtin_amdgcn_raw_buffer_load_b32(rws, zv8, zo + ((j >> 3) * 32 + (j & 7)) * 4096, AUX_SC1);
#pragma unroll
      for (int j = 0; j < 4; ++j) Z[16 + j] = __builtin_amdgcn_raw_buffer_load_b32(rws, zv4, zo + (64 + j) * 4096, AUX_SC1);
      sched0();
    }
  }
  TS(10);
  waitlgkm0(); bar();
#pragma unroll
  for (int pp = 0; pp < 3; ++pp)
#pragma unroll
    for (int T = 0; T < 5; ++T) acc[T] += *(LAS f32x4*)((own * 3 + pp) * 5120 + T * 1024 + lane * 16);
  const unsigned zsh = (c16 & 1) ? 0u : 16u;
  const float og = 1.f - gg;
  const unsigned qq[10] = {q0.x, q0.y, q0.z, q0.w, q1.x, q1.y, q1.z, q1.w, q2.x, q2.y};
  const unsigned ee[10] = {e0.x, e0.y, e0.z, e0.w, e1.x, e1.y, e1.z, e1.w, e2.x, e2.y};
  unsigned o[10];
#pragma unroll
  for (int j = 0; j < 10; ++j) {
    const float z0 = bhi(Z[2 * j] << zsh), z1 = bhi(Z[2 * j + 1] << zsh);
    const float b0 = acc[j >> 1][(j & 1) * 2] + blo(ee[j]), b1 = acc[j >> 1][(j & 1) * 2 + 1] + bhi(ee[j]);
    o[j] = pk2(gg * (blo(qq[j]) * z0) + og * b0, gg * (bhi(qq[j]) * z1) + og * b1);
  }
  *(u32x4*)(p.out + s * D + d0 + 8 * g) = u32x4{o[0], o[1], o[2], o[3]};
  *(u32x4*)(p.out + s * D + d0 + 32 + 8 * g) = u32x4{o[4], o[5], o[6], o[7]};
  *(u32x2*)(p.out + s * D + d0 + 64 + 4 * g) = u32x2{o[8], o[9]};
  TS(11);
  if (b == 255 && w == 0) {
#pragma unroll 1
    for (int n = 0; n < 5; ++n) flag_wait(rws, OFF_F2 + (n * 64 + lane) * 8, flag_load(rws, OFF_F2 + (n * 64 + lane) * 8), tag);
    flag_store(rws, OFF_E, tag);
  }
}

extern "C" void kernel_launch(void* const* d_in, const int* in_sizes, int n_in, void* d_out, int out_size, void* d_ws, size_t ws_size, hipStream_t stream) {
  Params p;
  p.q = (const u16*)d_in[0]; p.k = (const u16*)d_in[1]; p.v = (const u16*)d_in[2]; p.sk = (const u16*)d_in[3];
  p.fk = (const u16*)d_in[4]; p.ssd = (const u16*)d_in[5]; p.w1 = (const u16*)d_in[7]; p.b1 = (const u16*)d_in[8];
  p.w2 = (const u16*)d_in[9]; p.b2 = (const u16*)d_in[10]; p.gw = (const u16*)d_in[11];
  p.out = (u16*)d_out; p.ws = (char*)d_ws;
  (void)hipFuncSetAttribute((const void*)k_fused, hipFuncAttributeMaxDynamicSharedMemorySize, LDS_BYTES);
  k_fused<<<256, 256, LDS_BYTES, stream>>>(p);
}
```

```cpp
#include <hip/hip_runtime.h>
#include <stdint.h>

typedef unsigned short u16;
typedef short s16x8 __attribute__((ext_vector_type(8)));
typedef short s16x4 __attribute__((ext_vector_type(4)));
typedef float f32x4 __attribute__((ext_vector_type(4)));
typedef float f32x2 __attribute__((ext_vector_type(2)));
typedef __bf16 bf16x2 __attribute__((ext_vector_type(2)));
typedef unsigned u32x4 __attribute__((ext_vector_type(4)));
typedef unsigned u32x2 __attribute__((ext_vector_type(2)));
typedef int i32x4 __attribute__((ext_vector_type(4)));
#define LAS __attribute__((address_space(3)))
#define DEV __device__ __forceinline__
typedef __amdgpu_buffer_rsrc_t rsrc_t;

constexpr int S = 2048, D = 640, F = 1536;
constexpr unsigned RW_PITCH = 4096 + 128;
constexpr unsigned OFF_E = 0, OFF_F0 = 4096, OFF_F1 = 8192, OFF_F2 = 12288, OFF_FW = 16384;
constexpr unsigned OFF_RW = 1u << 20, OFF_W2T = 4u << 20, OFF_H = 6u << 20, OFF_Z = 13u << 20, OFF_G = 16u << 20, WS_USED = 17u << 20;
constexpr int LDS_BYTES = 160 * 1024;
constexpr unsigned L_CONV = 98304, CONV_CH = 17600, L_XCH = 133504, L_TO = 149888;
constexpr int AUX_SC1 = 16;
constexpr unsigned SPIN_CAP = 1u << 16;
constexpr unsigned CODE_BYTES = 28 * 1024;

struct Params {
  const u16 *q, *k, *v, *sk, *fk, *ssd, *w1, *b1, *w2, *b2, *gw;
  u16* out;
  char* ws;
};

DEV f32x4 mfma(u32x4 a, u32x4 b, f32x4 c) { return __builtin_amdgcn_mfma_f32_16x16x32_bf16(__builtin_bit_cast(s16x8, a), __builtin_bit_cast(s16x8, b), c, 0, 0, 0); }
DEV i32x4 mksrd(const void* p, unsigned bytes) {
  unsigned long long a = (unsigned long long)p;
  i32x4 r; r.x = (int)(unsigned)a; r.y = (int)(unsigned)(a >> 32); r.z = (int)bytes; r.w = 0x00020000; return r;
}
DEV rsrc_t mkrsrc(const void* p, unsigned bytes) { return __builtin_amdgcn_make_buffer_rsrc((void*)p, 0, (int)bytes, 0x00020000); }
template <int POL, int IMM> DEV void dma16(i32x4 srd, unsigned voff, unsigned soff, unsigned lds) {
  if (POL == 0) asm volatile("s_mov_b32 m0, %2\n\ts_nop 0\n\tbuffer_load_dwordx4 %0, %1, %3 offen offset:%4 lds" :: "v"(voff), "s"(srd), "s"(lds), "s"(soff), "i"(IMM) : "memory");
  if (POL == 1) asm volatile("s_mov_b32 m0, %2\n\ts_nop 0\n\tbuffer_load_dwordx4 %0, %1, %3 offen offset:%4 nt lds" :: "v"(voff), "s"(srd), "s"(lds), "s"(soff), "i"(IMM) : "memory");
  if (POL == 2) asm volatile("s_mov_b32 m0, %2\n\ts_nop 0\n\tbuffer_load_dwordx4 %0, %1, %3 offen offset:%4 sc1 lds" :: "v"(voff), "s"(srd), "s"(lds), "s"(soff), "i"(IMM) : "memory");
}
DEV void setm0(unsigned lds) { asm volatile("s_mov_b32 m0, %0" :: "s"(lds) : "memory"); }
template <int IMM> DEV void dma16m(i32x4 srd, unsigned voff, unsigned soff) {
  asm volatile("buffer_load_dwordx4 %0, %1, %2 offen offset:%3 lds" :: "v"(voff), "s"(srd), "s"(soff), "i"(IMM) : "memory");
}
template <int N> DEV void waitvm() { asm volatile("s_waitcnt vmcnt(%0)" :: "i"(N) : "memory"); }
DEV void waitlgkm0() { asm volatile("s_waitcnt lgkmcnt(0)" ::: "memory"); }
DEV void bar() { asm volatile("s_barrier" ::: "memory"); }
DEV void sched0() { __builtin_amdgcn_sched_barrier(0); }
DEV unsigned opq(unsigned x) { asm volatile("" : "+v"(x)); return x; }
DEV u32x4 lds128(unsigned a) { return *(LAS u32x4*)a; }
DEV u32x2 lds64(unsigned a) { return *(LAS u32x2*)a; }
DEV u32x2 ldstr(unsigned a) { return __builtin_bit_cast(u32x2, __builtin_amdgcn_ds_read_tr16_b64_v4i16((LAS s16x4*)a)); }
DEV unsigned pk2(float lo, float hi) { f32x2 z = {lo, hi}; return __builtin_bit_cast(unsigned, __builtin_convertvector(z, bf16x2)); }
DEV float blo(unsigned w) { return __builtin_bit_cast(float, w << 16); }
DEV float bhi(unsigned w) { return __builtin_bit_cast(float, w & 0xffff0000u); }
DEV float dot2(unsigned a, unsigned b, float c) { return __builtin_amdgcn_fdot2_f32_bf16(__builtin_bit_cast(bf16x2, a), __builtin_bit_cast(bf16x2, b), c, false); }

#define TS(idx) do {} while (0)
#define TSDECL
#define TSARG
#define TSPASS
#define TSFLUSH(lo, hi)

DEV u32x2 flag_load(rsrc_t rws, unsigned voff) { return __builtin_amdgcn_raw_buffer_load_b64(rws, voff, 0, AUX_SC1); }
DEV void flag_wait(rsrc_t rws, unsigned voff, u32x2 first, u32x2 tag) {
  if (__builtin_expect(__builtin_amdgcn_ballot_w64(first.x != tag.x || first.y != tag.y) != 0ull, 0)) {
    unsigned spins = 0;
    unsigned long long bad;
#pragma unroll 1
    do {
      __builtin_amdgcn_s_sleep(10);
      asm volatile("" ::: "memory");
      const u32x2 v = flag_load(rws, voff);
      bad = __builtin_amdgcn_ballot_w64(v.x != tag.x || v.y != tag.y);
    } while (bad != 0ull && ++spins < SPIN_CAP);
  }
  asm volatile("" ::: "memory");
}
DEV void flag_store(rsrc_t rws, unsigned off, u32x2 tag) {
  __builtin_amdgcn_raw_buffer_store_b64(tag, rws, threadIdx.x == 0 ? 0u : 0x80000000u, off, AUX_SC1);
}

DEV void phase_p0(const Params& p, rsrc_t rws, int hz, int w, int lane TSARG) {
  const i32x4 sk_ = mksrd(p.k, S * D * 2), sv_ = mksrd(p.v, S * D * 2);
  const int rt = hz >> 1, hf = hz & 1, s0 = rt * 64;
  const int g = lane >> 4, q4 = (lane >> 2) & 3, p4 = lane & 3, i16 = lane & 15;
  constexpr unsigned SLAB = 17408;
  const unsigned vrow = (lane >> 3) * 1280 + (((lane & 7) * 16) ^ ((g & 1) * 32)) + hf * 640;
  const unsigned vrA = vrow ^ ((w & 1) * 64), vrB = vrA ^ 64;
  dma16<1, 0>(sk_, vrow + (unsigned)((s0 - 8) * 1280) + w * 128, 0, w * SLAB);
  dma16<1, 0>(sk_, vrow + (unsigned)((s0 - 8) * 1280) + 4 * 128, 0, 4 * SLAB);
  u32x2 tap[5];
#pragma unroll
  for (int j = 0; j < 5; ++j) asm volatile("global_load_dwordx2 %0, %1, off" : "=v"(tap[j]) : "v"(p.sk + (hf * 320 + j * 64 + w * 16 + i16) * 4) : "memory");
  auto slab = [&](int j) {
    const unsigned so = (s0 + w * 16) * 1280 + j * 128;
    dma16<1, 0>(sk_, vrA, so, j * SLAB + 1024 + w * 2048);
    dma16<1, 0>(sk_, vrB, so + 8 * 1280, j * SLAB + 2048 + w * 2048);
    dma16<1, 0>(sv_, vrA, so, j * SLAB + 9216 + w * 2048);
    dma16<1, 0>(sv_, vrA, so + 8 * 1280, j * SLAB + 10240 + w * 2048);
  };
  slab(0); slab(1); slab(2);
  sched0();
  unsigned ka[4][2];
#pragma unroll
  for (int t = 0; t < 4; ++t)
#pragma unroll
    for (int mf = 0; mf < 2; ++mf) {
      const unsigned row = 8 + 16 * (i16 >> 2) + 4 * t + (i16 & 3) - 2 * mf - (g >> 1);
      ka[t][mf] = opq(row * 128 + ((w ^ (((row >> 1) & 1) | (((row >> 4) & 1) << 1))) * 32) + (g & 1) * 16 + 8192);
    }
  unsigned msk[4];
#pragma unroll
  for (int d = 0; d < 4; ++d) msk[d] = ((i16 >> 3) == (g & 1) && ((i16 & 7) >> 1) == d) ? (0xffffu << (16 * (i16 & 1))) : 0u;
  const unsigned sel = (g >> 1) ? 0x03020302u : 0x01000100u;
  const unsigned blk = (w ^ ((q4 >> 1) | ((g & 1) << 1))) * 32;
  const unsigned va = opq(9216 + (16 * g + q4) * 128 + blk + p4 * 8 + 8192);
  const unsigned ta = opq(L_TO + w * 2304 + i16 * 144 + (96 - 32 * g));
  const unsigned ra = opq(L_TO + w * 2304 + (lane >> 3) * 144 + (lane & 7) * 16);
  const unsigned vo = (lane >> 3) * RW_PITCH + (lane & 7) * 16;
  sched0();
  slab(3); slab(4);
  TS(1);
  u32x4 K[2][4][2]; u32x2 V[2][4];
  auto rd = [&](int j) {
    const unsigned jb = j * SLAB - 8192;
#pragma unroll
    for (int t = 0; t < 4; ++t) { K[j & 1][t][0] = lds128(ka[t][0] + jb); K[j & 1][t][1] = lds128(ka[t][1] + jb); V[j & 1][t] = ldstr(va + jb + t * 512); }
  };
  waitvm<16>(); bar();
  TS(2);
  rd(0);
  sched0();
#pragma unroll
  for (int j = 0; j < 5; ++j) {
    if (j < 4) {
      if (j == 0) waitvm<12>(); else if (j == 1) waitvm<8>(); else if (j == 2) waitvm<6>(); else waitvm<4>();
      bar();
      rd(j + 1);
      sched0();
    }
    asm volatile("" : "+v"(tap[j]));
    const unsigned r01 = __builtin_amdgcn_perm(tap[j].x, tap[j].x, sel), r23 = __builtin_amdgcn_perm(tap[j].y, tap[j].y, sel);
    const u32x4 B0 = {r01 & msk[0], r01 & msk[1], r01 & msk[2], r01 & msk[3]}, B1 = {r23 & msk[0], r23 & msk[1], r23 & msk[2], r23 & msk[3]};
    f32x4 ks[4];
#pragma unroll
    for (int t = 0; t < 4; ++t) ks[t] = mfma(K[j & 1][t][0], B0, f32x4{0.f, 0.f, 0.f, 0.f});
    sched0();
    u32x4 t0, t1;
    if (j > 0) { t0 = lds128(ra); t1 = lds128(ra + 8 * 144); }
#pragma unroll
    for (int t = 0; t < 4; ++t) ks[t] = mfma(K[j & 1][t][1], B1, ks[t]);
    sched0();
    float vf[4][4];
#pragma unroll
    for (int t = 0; t < 4; ++t) { vf[t][0] = blo(V[j & 1][t].x); vf[t][1] = bhi(V[j & 1][t].x); vf[t][2] = blo(V[j & 1][t].y); vf[t][3] = bhi(V[j & 1][t].y); }
    if (j > 0) {
      const unsigned so = OFF_RW + (hf * 320 + (j - 1) * 64 + w * 16) * RW_PITCH + (1984 - s0) * 2;
      __builtin_amdgcn_raw_buffer_store_b128(t0, rws, vo, so, AUX_SC1);
      __builtin_amdgcn_raw_buffer_store_b128(t1, rws, vo, so + 8 * RW_PITCH, AUX_SC1);
    }
    sched0();
    unsigned r[8];
#pragma unroll
    for (int t = 0; t < 4; ++t) {
      r[7 - 2 * t] = pk2(ks[t][1] * vf[t][1], ks[t][0] * vf[t][0]);
      r[6 - 2 * t] = pk2(ks[t][3] * vf[t][3], ks[t][2] * vf[t][2]);
    }
    *(LAS u32x4*)ta = u32x4{r[0], r[1], r[2], r[3]}; *(LAS u32x4*)(ta + 16) = u32x4{r[4], r[5], r[6], r[7]};
    sched0();
    TS(3 + j);
  }
  {
    const u32x4 t0 = lds128(ra), t1 = lds128(ra + 8 * 144);
    const unsigned so = OFF_RW + (hf * 320 + 4 * 64 + w * 16) * RW_PITCH + (1984 - s0) * 2;
    __builtin_amdgcn_raw_buffer_store_b128(t0, rws, vo, so, AUX_SC1);
    __builtin_amdgcn_raw_buffer_store_b128(t1, rws, vo, so + 8 * RW_PITCH, AUX_SC1);
    __builtin_amdgcn_raw_buffer_store_b128(u32x4{0u, 0u, 0u, 0u}, rws, threadIdx.x < 10 ? threadIdx.x * RW_PITCH + 4096 : 0x80000000u, OFF_RW + hz * 10 * RW_PITCH, AUX_SC1);
  }
}

DEV void phase_gate(const Params& p, rsrc_t rws, int row0, int w, int lane) {
  const int g = lane >> 4, i16 = lane & 15;
  u32x4 gw[5], x[2][5];
#pragma unroll
  for (int n = 0; n < 5; ++n) gw[n] = *(const u32x4*)(p.gw + (n * 16 + i16) * 8);
#pragma unroll
  for (int r = 0; r < 2; ++r)
#pragma unroll
    for (int n = 0; n < 5; ++n) x[r][n] = *(const u32x4*)(p.q + (row0 + w * 8 + r * 4 + g) * D + (n * 16 + i16) * 8);
#pragma unroll
  for (int r = 0; r < 2; ++r) {
    float a = 0.f;
#pragma unroll
    for (int n = 0; n < 5; ++n) { a = dot2(x[r][n].x, gw[n].x, a); a = dot2(x[r][n].y, gw[n].y, a); a = dot2(x[r][n].z, gw[n].z, a); a = dot2(x[r][n].w, gw[n].w, a); }
    a += __shfl_xor(a, 1); a += __shfl_xor(a, 2); a += __shfl_xor(a, 4); a += __shfl_xor(a, 8);
    const float gg = __builtin_amdgcn_rcpf(1.f + __builtin_amdgcn_exp2f(-1.4426950408889634f * a));
    __builtin_amdgcn_raw_buffer_store_b32(__builtin_bit_cast(unsigned, gg), rws, i16 == 0 ? (unsigned)(row0 + w * 8 + r * 4 + g) * 4 : 0x80000000u, OFF_G, AUX_SC1);
  }
}

DEV void conv_pads(unsigned lbase, int w, int lane) {
  const unsigned cbase = lbase + (w >> 1) * CONV_CH, half = w & 1;
  const unsigned rb = cbase + half * 4576, fb = cbase + 9152 + half * 4224;
  const int l = lane & 31;
  *(LAS u32x4*)(l < 28 ? rb + 4096 + l * 16 : fb + (l - 28) * 16) = u32x4{0u, 0u, 0u, 0u};
}
template <int N> DEV void conv_load_f(const Params& p, int c0, unsigned lbase, int w, int lane) {
  const int ch = c0 + (w >> 1), half = w & 1;
  dma16<1, N * 1024>(mksrd(p.fk, D * S * 2), lane * 16, ch * 4096, lbase + (w >> 1) * CONV_CH + 9152 + half * 4224 + 60 + half * 4);
}
DEV void conv_load_r(const Params& p, int c0, unsigned lbase, int w, int lane) {
  const int ch = c0 + (w >> 1), half = w & 1;
  const i32x4 sr = mksrd(p.ws, WS_USED);
  const unsigned so = OFF_RW + ch * RW_PITCH + 2 - 2 * half, lb = lbase + (w >> 1) * CONV_CH + half * 4576;
  dma16<2, 0>(sr, lane * 16, so, lb); dma16<2, 1024>(sr, lane * 16, so, lb); dma16<2, 2048>(sr, lane * 16, so, lb); dma16<2, 3072>(sr, lane * 16, so, lb);
}

DEV void conv_load_r1(const Params& p, int c0, unsigned lbase, int w, int lane, int n) {
  const int ch = c0 + (w >> 1), half = w & 1;
  const i32x4 sr = mksrd(p.ws, WS_USED);
  const unsigned so = OFF_RW + ch * RW_PITCH + 2 - 2 * half, lb = lbase + (w >> 1) * CONV_CH + half * 4576;
  if (n == 0) dma16<2, 0>(sr, lane * 16, so, lb);
  if (n == 1) dma16<2, 1024>(sr, lane * 16, so, lb);
  if (n == 2) dma16<2, 2048>(sr, lane * 16, so, lb);
  if (n == 3) dma16<2, 3072>(sr, lane * 16, so, lb);
}

struct G2Ctx { unsigned wbase, hbase, f1_off, fw_off; };
DEV u32x4 load_w(rsrc_t rws, const G2Ctx& c, int lane, int n) {
  return __builtin_amdgcn_raw_buffer_load_b128(rws, lane * 16 + (n & 3) * 1024, c.wbase + (n >> 2) * 4096, AUX_SC1);
}
DEV u32x4 load_h(rsrc_t rws, const G2Ctx& c, int w, int lane, int n) {
  return __builtin_amdgcn_raw_buffer_load_b128(rws, lane * 16 + (n & 3) * 1024, c.hbase + (((w + 1 + n / 12) & 3) * 48 + ((n % 12) & ~3)) * 1024, AUX_SC1);
}

template <int W0, int NW, bool LAST>
DEV void conv_compute(const Params& p, rsrc_t rws, unsigned lbase, unsigned dbits_in, int w, int lane, const G2Ctx& c2, u32x4 (&W)[60], u32x4 (&H)[48],
                      u32x2 tag, unsigned pub_off, f32x4 (&acc)[8] TSARG) {
  const int half = w & 1, g = lane >> 4, n16 = lane & 15;
  const unsigned cbase = lbase + (w >> 1) * CONV_CH;
  const unsigned ua = opq(cbase + (n16 & 1) * 4576 + 448 - 64 * (n16 >> 1) + 16 * g + 256 * half);
  const unsigned fa = opq(cbase + 9152 + (n16 & 1) * 4228 + 4 * n16 + 16 * g + 256 * half);
  const unsigned fa2 = opq(fa + 8);
  const unsigned dbits = half ? 0u : dbits_in;
  const int dsel = 15 - n16 - 4 * g;
  const u32x4 Df = {dsel == 0 ? dbits : 0u, dsel == 1 ? dbits : 0u, dsel == 2 ? dbits : 0u, dsel == 3 ? dbits : 0u};
  u32x4 U[2][8]; u32x2 Fl[2][8], Fh[2][8];
  u32x2 poll;
#pragma unroll
  for (int j = 0; j < 8; ++j) { U[0][j] = lds128(ua + 512 * (7 - j)); sched0(); }
#pragma unroll
  for (int j = 0; j < 8; ++j) {
    Fl[0][j] = lds64(fa + 512 * j); Fh[0][j] = lds64(fa2 + 512 * j); sched0();
    acc[j] = mfma(U[0][j], Df, f32x4{0.f, 0.f, 0.f, 0.f}); sched0();
  }
  int wn = 0;
#pragma unroll
  for (int r = 0; r < 4; ++r) {
    const int cur = r & 1, nxt = cur ^ 1;
    int m = 0;
#pragma unroll
    for (int E = 0; E < 8; ++E)
#pragma unroll
      for (int I = E; I < 8; ++I) {
        acc[I] = mfma(U[cur][I - E], u32x4{Fl[cur][E].x, Fl[cur][E].y, Fh[cur][E].x, Fh[cur][E].y}, acc[I]);
        sched0();
        if (r < 3 && m % 3 != 2) {
          const int x = (m / 3) * 2 + (m % 3);
          if (x < 8) U[nxt][x] = lds128(ua + 512 * (7 - x) + 64 * (r + 1));
          else if (x & 1) Fh[nxt][(x - 8) >> 1] = lds64(fa2 + 512 * ((x - 8) >> 1) + 64 * (r + 1));
          else Fl[nxt][(x - 8) >> 1] = lds64(fa + 512 * ((x - 8) >> 1) + 64 * (r + 1));
          sched0();
        }
        if (m % 3 == 2) {
          if (LAST) {
            if (r < 3 && wn < NW) { W[W0 + wn] = load_w(rws, c2, lane, W0 + wn); ++wn; sched0(); }
            if (r == 2 && m == 14) { poll = flag_load(rws, c2.f1_off); sched0(); }
            if (r == 3) { H[m / 3] = load_h(rws, c2, w, lane, m / 3); sched0(); }
          } else if (wn < NW && m < 24) { W[W0 + wn] = load_w(rws, c2, lane, W0 + wn); ++wn; sched0(); }
        }
        ++m;
      }
    if (LAST && r == 0) { waitvm<12>(); bar(); flag_store(rws, pub_off, tag); sched0(); }
    if (LAST && r == 2) { TS(14); flag_wait(rws, c2.f1_off, poll, tag); TS(15); }
  }
}
DEV void conv_finish(rsrc_t rws, int c0, int w, int lane, f32x4 (&acc)[8]) {
  const int ch = c0 + (w >> 1), half = w & 1;
  const unsigned xw = L_XCH + w * 4096 + lane * 16, xr = L_XCH + (w ^ 1) * 4096 + lane * 16;
  if (half == 0) {
#pragma unroll
    for (int i = 0; i < 4; ++i) *(LAS f32x4*)(xw + i * 1024) = acc[4 + i];
  } else {
#pragma unroll
    for (int i = 0; i < 4; ++i) { *(LAS f32x4*)(xw + i * 1024) = acc[i]; acc[i] = acc[4 + i]; }
  }
  waitlgkm0(); bar();
  f32x4 oth[4];
#pragma unroll
  for (int i = 0; i < 4; ++i) oth[i] = *(LAS f32x4*)(xr + i * 1024);
#pragma unroll
  for (int i = 0; i < 4; ++i) {
    const f32x4 y = acc[i] + oth[i];
    const u32x2 o = {pk2(y[0], y[1]), pk2(y[2], y[3])};
    __builtin_amdgcn_raw_buffer_store_b64(o, rws, lane * 8, OFF_Z + ch * 4096 + (half * 4 + i) * 512, AUX_SC1);
  }
  sched0();
}

DEV f32x2 gelu2(f32x2 z) {
  const float c0 = -2.f * 0.7978845608028654f * 1.4426950408889634f, c1 = c0 * 0.044715f;
  const f32x2 a = z * __builtin_elementwise_fma(z * z, f32x2{c1, c1}, f32x2{c0, c0});
  const f32x2 d = f32x2{__builtin_amdgcn_exp2f(a.x), __builtin_amdgcn_exp2f(a.y)} + f32x2{1.f, 1.f};
  return z * f32x2{__builtin_amdgcn_rcpf(d.x), __builtin_amdgcn_rcpf(d.y)};
}
DEV unsigned gelu_pk(float a, float b) { const f32x2 r = gelu2(f32x2{a, b}); return pk2(r.x, r.y); }

DEV void phase_g1(const Params& p, rsrc_t rws, int rt, int ct, int off, int xs, int gi, int c0, int w, int lane, const G2Ctx& c2, u32x4 (&W)[60], u32x2& tag TSARG) {
  const i32x4 sx = mksrd(p.q, S * D * 2), sw = mksrd(p.w1, D * F * 2);
  const int g = lane >> 4, q4 = (lane >> 2) & 3, p4 = lane & 3, n16 = lane & 15, wf = w & 1, wsn = w >> 1;
  unsigned vx[4], vw[4];
#pragma unroll
  for (int j = 0; j < 4; ++j) {
    vx[j] = (((rt * 128 + w * 32 + (lane >> 3)) * 1280 + ((lane & 7) ^ g) * 16) ^ ((j & 1) * 64)) + j * (8 * 1280 - 1024);
    vw[j] = (((w * 16 + g) * 3072 + ct * 256 + (n16 ^ (g << 1)) * 16) ^ ((j >> 1) * 128)) + j * (4 * 3072 - 1024);
  }
  auto kstage = [&](int st) {
    int ks = (st & ~3) + ((st + off) & (st < 8 ? 3 : 1)) + xs; if (ks >= 10) ks -= 10; return ks;
  };
  auto prep = [&](int st, int op) { setm0((st % 3) * 32768 + w * 4096 + op * 16384); };
  auto issue = [&](int st, int j) {
    const int ks = kstage(st);
    if (j == 0) dma16m<0>(sx, vx[0], ks * 128);
    if (j == 1) dma16m<1024>(sx, vx[1], ks * 128);
    if (j == 2) dma16m<2048>(sx, vx[2], ks * 128);
    if (j == 3) dma16m<3072>(sx, vx[3], ks * 128);
    if (j == 4) dma16m<0>(sw, vw[0], ks * 64 * 3072);
    if (j == 5) dma16m<1024>(sw, vw[1], ks * 64 * 3072);
    if (j == 6) dma16m<2048>(sw, vw[2], ks * 64 * 3072);
    if (j == 7) dma16m<3072>(sw, vw[3], ks * 64 * 3072);
  };
  auto stage_all = [&](int st) {
    prep(st, 0); asm volatile("s_nop 0"); issue(st, 0); issue(st, 1); issue(st, 2); issue(st, 3);
    prep(st, 1); asm volatile("s_nop 0"); issue(st, 4); issue(st, 5); issue(st, 6); issue(st, 7);
  };
  stage_all(0);
  u32x2 bias[4];
#pragma unroll
  for (int a = 0; a < 4; ++a) asm volatile("global_load_dwordx2 %0, %1, off" : "=v"(bias[a]) : "v"(p.b1 + ct * 128 + wf * 64 + a * 16 + g * 4) : "memory");
  asm volatile("buffer_load_dwordx2 %0, off, %1, 0 sc1" : "=v"(tag) : "s"(mksrd(p.ws, WS_USED)) : "memory");
  {
    const i32x4 sw2 = mksrd(p.w2, F * D * 2);
#pragma unroll
    for (int n = 0; n < 3; ++n) {
      const unsigned P = (w * 3 + n) * 64 + lane, row = (P * 205) >> 12;
      dma16<1, 0>(sw2, P < 640 ? row * (1280 - 320) + P * 16 : 0x80000000u, (gi >> 2) * 40960 + (gi & 3) * 320, L_XCH + (w * 3 + n) * 1024);
    }
  }
  stage_all(1);
  conv_pads(L_CONV, w, lane);
  waitlgkm0();
  conv_load_f<0>(p, c0, L_CONV, w, lane); conv_load_f<1>(p, c0, L_CONV, w, lane); conv_load_f<2>(p, c0, L_CONV, w, lane); conv_load_f<3>(p, c0, L_CONV, w, lane);
  unsigned wa[2][4], xa[2][2];
#pragma unroll
  for (int a = 0; a < 4; ++a) {
    wa[0][a] = opq(16384 + (8 * g + q4) * 256 + p4 * 8 + ((((wf ^ (g & 1)) << 2) | (a ^ q4)) * 32));
    wa[1][a] = opq(wa[0][a] + 65536);
  }
  xa[0][0] = opq((wsn * 64 + n16) * 128 + ((g ^ (n16 >> 1)) * 16)); xa[0][1] = opq(xa[0][0] ^ 64);
  xa[1][0] = opq(xa[0][0] + 65536); xa[1][1] = opq(xa[0][1] + 65536);
  unsigned ta[3], to[3];
#pragma unroll
  for (int n = 0; n < 3; ++n) {
    const int fi = w + 4 * n, cbl = fi >= 5, T = fi - 5 * cbl;
    ta[n] = opq(L_XCH + (4 * g + q4) * 320 + cbl * 160 + (T < 4 ? 64 * (T >> 1) + 8 * (T & 1) + 16 * p4 : 128 + 8 * p4));
    to[n] = fi < 10 ? OFF_W2T + ((((gi & 3) * 2 + cbl) * 48 + (gi >> 2)) * 5 + T) * 1024 : 0x80000000u;
  }
  f32x4 acc[4][4];
  u32x2 Al[2][4], Ah[2][4], Tl[3], Th[3]; u32x4 B[2][4];
  auto rd = [&](int buf, int st, int kk, int x) {
    const int sl = st % 3, hi = sl == 2;
    const unsigned base = (sl & 1) * 32768;
    constexpr int kind[12] = {1, 2, 0, 0, 1, 0, 2, 0, 1, 2, 1, 2}, idx[12] = {0, 0, 0, 1, 1, 2, 1, 3, 2, 2, 3, 3};
    if (kind[x] == 0) B[buf][idx[x]] = lds128(xa[hi][kk] + base + idx[x] * 2048);
    if (kind[x] == 1) Al[buf][idx[x]] = ldstr(wa[hi][idx[x]] + base + kk * 8192);
    if (kind[x] == 2) Ah[buf][idx[x]] = ldstr(wa[hi][idx[x]] + base + kk * 8192 + 1024);
  };
  waitvm<12>(); bar();
  TS(1);
#pragma unroll
  for (int x = 0; x < 12; ++x) rd(0, 0, 0, x);
  prep(2, 0); asm volatile("s_nop 0"); issue(2, 0); issue(2, 1); issue(2, 2); issue(2, 3);
#pragma unroll
  for (int a = 0; a < 4; ++a) {
    asm volatile("" : "+v"(bias[a]));
    const f32x4 bv = {blo(bias[a].x), bhi(bias[a].x), blo(bias[a].y), bhi(bias[a].y)};
#pragma unroll
    for (int b = 0; b < 4; ++b) acc[a][b] = bv;
  }
  asm volatile("v_add_u32 %0, 1, %0" : "+v"(tag.x), "+v"(tag.y));
  sched0();
  u32x2 pollk, pollw;
  int wn = 0;
#pragma unroll
  for (int st = 0; st < 10; ++st) {
#pragma unroll
    for (int m = 0; m < 16; ++m) {
      const int a = m >> 2, b = m & 3;
      acc[a][b] = mfma(u32x4{Al[0][a].x, Al[0][a].y, Ah[0][a].x, Ah[0][a].y}, B[0][b], acc[a][b]);
      sched0();
      if (m < 12) { rd(1, st, 1, m); sched0(); }
      if (st + 2 < 10) {
        if (m == 0) { prep(st + 2, 1); sched0(); }
        if ((m & 3) == 1) { issue(st + 2, 4 + (m >> 2)); sched0(); }
      }
      if (st == 0 && m >= 12 && m < 15) { Tl[m - 12] = ldstr(ta[m - 12]); Th[m - 12] = ldstr(ta[m - 12] + 16 * 320); sched0(); }
      if (st == 9 && (m & 3) == 1) { W[wn] = load_w(rws, c2, lane, wn); ++wn; sched0(); }
    }
    if (st < 9) {
      waitlgkm0();
      if (st == 1) waitvm<11>(); else if (st == 3) waitvm<9>(); else if (st == 8) waitvm<2>(); else if (st == 0) waitvm<12>(); else waitvm<8>();
      bar();
      if (st == 2) { flag_store(rws, OFF_FW + ((gi & 3) * 64 + (gi >> 2)) * 8, tag); sched0(); }
      if (st == 8) {
        flag_wait(rws, OFF_F0 + lane * 8, pollk, tag);
        flag_wait(rws, c2.fw_off, pollw, tag);
        TS(11);
      }
    }
#pragma unroll
    for (int m = 0; m < 16; ++m) {
      const int a = m >> 2, b = m & 3;
      acc[a][b] = mfma(u32x4{Al[1][a].x, Al[1][a].y, Ah[1][a].x, Ah[1][a].y}, B[1][b], acc[a][b]);
      if (st < 9) {
        sched0();
        if (m < 12) { rd(0, st + 1, 0, m); sched0(); }
        if (st + 3 < 10) {
          if (m == 0) { prep(st + 3, 0); sched0(); }
          if ((m & 3) == 1) { issue(st + 3, m >> 2); sched0(); }
        }
        if (st == 0 && m >= 12 && m < 15) {
          __builtin_amdgcn_raw_buffer_store_b128(u32x4{Tl[m - 12].x, Tl[m - 12].y, Th[m - 12].x, Th[m - 12].y}, rws, lane * 16 + to[m - 12], 0, AUX_SC1); sched0();
        }
        if (st == 7 && m == 5) { pollk = flag_load(rws, OFF_F0 + lane * 8); sched0(); }
        if (st == 7 && m == 9) { pollw = flag_load(rws, c2.fw_off); sched0(); }
        if (st == 8 && (m & 3) == 1) { conv_load_r1(p, c0, L_CONV, w, lane, m >> 2); sched0(); }
        if (st == 8 && (m & 3) == 3) { W[wn] = load_w(rws, c2, lane, wn); ++wn; sched0(); }
      } else if ((m & 3) == 3) { sched0(); W[wn] = load_w(rws, c2, lane, wn); ++wn; sched0(); }
    }
  }
  TS(12);
#pragma unroll
  for (int n = 0; n < 8; ++n) {
    const int b = n >> 1, P = n & 1;
    constexpr float c0f = -2.f * 0.7978845608028654f * 1.4426950408889634f, c1f = c0f * 0.044715f;
    f32x2 z[4], t[4];
    z[0] = f32x2{acc[2 * P][b][0], acc[2 * P][b][1]}; z[1] = f32x2{acc[2 * P][b][2], acc[2 * P][b][3]};
    z[2] = f32x2{acc[2 * P + 1][b][0], acc[2 * P + 1][b][1]}; z[3] = f32x2{acc[2 * P + 1][b][2], acc[2 * P + 1][b][3]};
#pragma unroll
    for (int x = 0; x < 4; ++x) asm volatile("" : "+v"(z[x]));
    sched0();
#pragma unroll
    for (int x = 0; x < 4; ++x) t[x] = z[x] * z[x];
    sched0();
#pragma unroll
    for (int x = 0; x < 4; ++x) t[x] = __builtin_elementwise_fma(t[x], f32x2{c1f, c1f}, f32x2{c0f, c0f});
    sched0();
    if (n < 7) { W[wn] = load_w(rws, c2, lane, wn); ++wn; sched0(); }
#pragma unroll
    for (int x = 0; x < 4; ++x) t[x] = t[x] * z[x];
    sched0();
#pragma unroll
    for (int x = 0; x < 4; ++x) t[x] = f32x2{__builtin_amdgcn_exp2f(t[x].x), __builtin_amdgcn_exp2f(t[x].y)};
    sched0();
    if (n < 7) { W[wn] = load_w(rws, c2, lane, wn); ++wn; sched0(); }
#pragma unroll
    for (int x = 0; x < 4; ++x) t[x] = t[x] + f32x2{1.f, 1.f};
    sched0();
#pragma unroll
    for (int x = 0; x < 4; ++x) t[x] = f32x2{__builtin_amdgcn_rcpf(t[x].x), __builtin_amdgcn_rcpf(t[x].y)};
    sched0();
    if (n < 7) { W[wn] = load_w(rws, c2, lane, wn); ++wn; sched0(); }
#pragma unroll
    for (int x = 0; x < 4; ++x) t[x] = t[x] * z[x];
    sched0();
    const u32x4 o = {pk2(t[0].x, t[0].y), pk2(t[1].x, t[1].y), pk2(t[2].x, t[2].y), pk2(t[3].x, t[3].y)};
    sched0();
    if (n < 7) { W[wn] = load_w(rws, c2, lane, wn); ++wn; sched0(); }
    __builtin_amdgcn_raw_buffer_store_b128(o, rws, lane * 16, OFF_H + ((rt * 8 + wsn * 4 + b) * 48 + ct * 4 + wf * 2 + P) * 1024, AUX_SC1);
    sched0();
  }
  waitvm<45>(); bar();
}

__global__ __launch_bounds__(256, 1) void k_fused(Params p) {
  extern __shared__ __attribute__((aligned(16))) char smem[];
  const int b = blockIdx.x, xcd = b & 7, i = b >> 3;
  const int w = __builtin_amdgcn_readfirstlane(threadIdx.x >> 6), lane = threadIdx.x & 63;
  TSDECL; TS(0);
  unsigned pf;
  {
    unsigned long long pc; asm volatile("s_getpc_b64 %0" : "=s"(pc));
    const i32x4 sc = mksrd((const void*)(pc & ~127ull), CODE_BYTES);
    asm volatile("buffer_load_dword %0, %1, %2, 0 offen" : "=v"(pf) : "v"(threadIdx.x < 8 ? (unsigned)(i * 1024 + threadIdx.x * 128) : 0x80000000u), "s"(sc) : "memory");
  }
  const rsrc_t rws = mkrsrc(p.ws, WS_USED), rsd = mkrsrc(p.ssd, D * 2);
  u32x2 tag;
  const int ri = i >> 3, cb = i & 7, R = xcd * 4 + ri;
  G2Ctx c2;
  c2.wbase = OFF_W2T + (cb * 48 + w * 12) * 5120; c2.hbase = OFF_H + (R * 4 * 48 + w * 12) * 1024;
  c2.f1_off = OFF_F1 + ((R >> 1) * 12 + (lane < 12 ? lane : 0)) * 8;
  c2.fw_off = OFF_FW + ((cb >> 1) * 64 + (lane < 48 ? lane : 0)) * 8;
  u32x4 W[60], H[48];
  f32x4 cacc[8];
  int c0; unsigned pub_off, dbits;
  if (i >= 24) {
    const int hz = (i - 24) * 8 + xcd;
    const int cA = (192 + 2 * hz) * 2;
    c0 = cA + 2;
    tag = flag_load(rws, OFF_E);
    const unsigned dA = __builtin_amdgcn_raw_buffer_load_b16(rsd, 0, (cA + (w >> 1)) * 2, 0);
    dbits = __builtin_amdgcn_raw_buffer_load_b16(rsd, 0, (c0 + (w >> 1)) * 2, 0);
    sched0();
    phase_p0(p, rws, hz, w, lane TSPASS);
    TS(9);
    waitvm<0>(); waitlgkm0(); bar();
    asm volatile("" :: "v"(pf));
    tag.x = opq(tag.x) + 1u;
    flag_store(rws, OFF_F0 + hz * 8, tag);
    TS(11);
    conv_pads(0, w, lane); conv_pads(L_CONV, w, lane);
    waitlgkm0();
    conv_load_f<0>(p, cA, 0, w, lane); conv_load_f<1>(p, cA, 0, w, lane); conv_load_f<2>(p, cA, 0, w, lane); conv_load_f<3>(p, cA, 0, w, lane);
    conv_load_f<0>(p, c0, L_CONV, w, lane); conv_load_f<1>(p, c0, L_CONV, w, lane); conv_load_f<2>(p, c0, L_CONV, w, lane); conv_load_f<3>(p, c0, L_CONV, w, lane);
    phase_gate(p, rws, xcd * 256 + (i - 24) * 32, w, lane);
    sched0();
    {
      const u32x2 pk = flag_load(rws, OFF_F0 + lane * 8), pw = flag_load(rws, c2.fw_off);
      flag_wait(rws, OFF_F0 + lane * 8, pk, tag);
      flag_wait(rws, c2.fw_off, pw, tag);
    }
    TS(12);
    conv_load_r(p, cA, 0, w, lane); conv_load_r(p, c0, L_CONV, w, lane);
#pragma unroll
    for (int n = 0; n < 8; ++n) W[n] = load_w(rws, c2, lane, n);
    sched0();
    waitvm<12>(); bar();
    conv_compute<8, 32, false>(p, rws, 0, dA, w, lane, c2, W, H, tag, 0, cacc TSPASS);
    conv_finish(rws, cA, w, lane, cacc);
    waitvm<44>(); bar();
    pub_off = OFF_F2 + (192 + 2 * hz) * 8;
  } else {
    const int r = (i * 43) >> 8, c = i - r * 6, gi = i * 8 + xcd;
    const int rt = (xcd >> 1) * 4 + r, ct = (xcd & 1) * 6 + c;
    c0 = gi * 2;
    dbits = __builtin_amdgcn_raw_buffer_load_b16(rsd, 0, (c0 + (w >> 1)) * 2, 0);
    sched0();
    phase_g1(p, rws, rt, ct, c + r, xcd + (xcd >> 2), gi, c0, w, lane, c2, W, tag TSPASS);
    asm volatile("" :: "v"(pf));
    pub_off = OFF_F1 + (rt * 12 + ct) * 8;
  }
  TS(13);
  conv_compute<40, 20, true>(p, rws, L_CONV, dbits, w, lane, c2, W, H, tag, pub_off, cacc TSPASS);
  TS(16);
  conv_finish(rws, c0, w, lane, cacc);
  TS(17);
  constexpr int LA = 12;
  const int g = lane >> 4, c16 = lane & 15, own = w;
  const int s = R * 64 + own * 16 + c16, d0 = cb * 80;
  const int hzA = (2 * ri) * 8 + xcd;
  const unsigned f2_off = OFF_F2 + (lane < 40 ? cb * 40 + lane : 192 + 2 * (hzA + (lane & 1) * 8)) * 8;
  f32x4 ac2[2][5], part[3][5];
  u32x4 q0, q1, e0, e1; u32x2 q2, e2, poll; unsigned Z[20]; float gg;
  const unsigned pr = opq(own * 15360 + lane * 16);
  sched0();
#pragma unroll
  for (int pp = 0; pp < 4; ++pp) {
    f32x4 (&acc)[5] = ac2[pp & 1];
#pragma unroll
    for (int ii = 0; ii < 12; ++ii) {
      const int n = pp * 12 + ii;
#pragma unroll
      for (int T = 0; T < 5; ++T) {
        acc[T] = mfma(W[ii * 5 + T], H[n], ii == 0 ? f32x4{0.f, 0.f, 0.f, 0.f} : acc[T]);
        sched0();
        if (T == 1 && n + LA < 48) { H[n + LA] = load_h(rws, c2, w, lane, n + LA); sched0(); }
        if (T == 3 && pp > 0 && ii < 5) { *(LAS f32x4*)((((own + pp) & 3) * 3 + pp - 1) * 5120 + ii * 1024 + lane * 16) = ac2[(pp - 1) & 1][ii]; sched0(); }
        if (pp == 3 && ii >= 7 && ii < 10) { part[ii - 7][T] = *(LAS f32x4*)(pr + (ii - 7) * 5120 + T * 1024); sched0(); }
      }
      if (n == 7) { waitvm<8>(); bar(); flag_store(rws, OFF_F2 + (c0 >> 1) * 8, tag); sched0(); }
      if (n == 30) { poll = flag_load(rws, f2_off); sched0(); }
      if (n == 42) { waitlgkm0(); bar(); }
    }
    if (pp == 2) {
      TS(18);
      q0 = *(const u32x4*)(p.q + s * D + d0 + 8 * g); q1 = *(const u32x4*)(p.q + s * D + d0 + 32 + 8 * g);
      q2 = *(const u32x2*)(p.q + s * D + d0 + 64 + 4 * g);
      e0 = *(const u32x4*)(p.b2 + d0 + 8 * g); e1 = *(const u32x4*)(p.b2 + d0 + 32 + 8 * g);
      e2 = *(const u32x2*)(p.b2 + d0 + 64 + 4 * g);
      flag_wait(rws, f2_off, poll, tag);
      TS(19);
      gg = __builtin_bit_cast(float, __builtin_amdgcn_raw_buffer_load_b32(rws, s * 4, OFF_G, AUX_SC1));
      const unsigned zo = OFF_Z + d0 * 4096 + R * 128 + (own >> 1) * 4 + (own & 1) * 64;
      const unsigned zv8 = g * 8 * 4096 + (c16 >> 1) * 8, zv4 = g * 4 * 4096 + (c16 >> 1) * 8;
#pragma unroll
      for (int j = 0; j < 16; ++j) Z[j] = __builtin_amdgcn_raw_buffer_load_b32(rws, zv8, zo + ((j >> 3) * 32 + (j & 7)) * 4096, AUX_SC1);
#pragma unroll
      for (int j = 0; j < 4; ++j) Z[16 + j] = __builtin_amdgcn_raw_buffer_load_b32(rws, zv4, zo + (64 + j) * 4096, AUX_SC1);
      sched0();
    }
  }
  TS(20);
  f32x4 (&acc)[5] = ac2[1];
#pragma unroll
  for (int T = 0; T < 5; ++T) acc[T] += (part[0][T] + part[1][T]) + part[2][T];
  const unsigned zsh = (c16 & 1) ? 0u : 16u;
  const float og = 1.f - gg;
  const unsigned qq[10] = {q0.x, q0.y, q0.z, q0.w, q1.x, q1.y, q1.z, q1.w, q2.x, q2.y};
  const unsigned ee[10] = {e0.x, e0.y, e0.z, e0.w, e1.x, e1.y, e1.z, e1.w, e2.x, e2.y};
  unsigned o[10];
#pragma unroll
  for (int j = 0; j < 10; ++j) {
    const float z0 = bhi(Z[2 * j] << zsh), z1 = bhi(Z[2 * j + 1] << zsh);
    const float b0 = acc[j >> 1][(j & 1) * 2] + blo(ee[j]), b1 = acc[j >> 1][(j & 1) * 2 + 1] + bhi(ee[j]);
    o[j] = pk2(gg * (blo(qq[j]) * z0) + og * b0, gg * (bhi(qq[j]) * z1) + og * b1);
  }
  *(u32x4*)(p.out + s * D + d0 + 8 * g) = u32x4{o[0], o[1], o[2], o[3]};
  *(u32x4*)(p.out + s * D + d0 + 32 + 8 * g) = u32x4{o[4], o[5], o[6], o[7]};
  *(u32x2*)(p.out + s * D + d0 + 64 + 4 * g) = u32x2{o[8], o[9]};
  TS(21);
  if (b == 255 && w == 0) {
#pragma unroll 1
    for (int n = 0; n < 5; ++n) flag_wait(rws, OFF_F2 + (n * 64 + lane) * 8, flag_load(rws, OFF_F2 + (n * 64 + lane) * 8), tag);
    flag_store(rws, OFF_E, tag);
  }
}

extern "C" void kernel_launch(void* const* d_in, const int* in_sizes, int n_in, void* d_out, int out_size, void* d_ws, size_t ws_size, hipStream_t stream) {
  Params p;
  p.q = (const u16*)d_in[0]; p.k = (const u16*)d_in[1]; p.v = (const u16*)d_in[2]; p.sk = (const u16*)d_in[3];
  p.fk = (const u16*)d_in[4]; p.ssd = (const u16*)d_in[5]; p.w1 = (const u16*)d_in[7]; p.b1 = (const u16*)d_in[8];
  p.w2 = (const u16*)d_in[9]; p.b2 = (const u16*)d_in[10]; p.gw = (const u16*)d_in[11];
  p.out = (u16*)d_out; p.ws = (char*)d_ws;
  (void)hipFuncSetAttribute((const void*)k_fused, hipFuncAttributeMaxDynamicSharedMemorySize, LDS_BYTES);
  k_fused<<<256, 256, LDS_BYTES, stream>>>(p);
}
```

```cpp
#include <hip/hip_runtime.h>
#include <stdint.h>

typedef unsigned short u16;
typedef short s16x8 __attribute__((ext_vector_type(8)));
typedef short s16x4 __attribute__((ext_vector_type(4)));
typedef float f32x4 __attribute__((ext_vector_type(4)));
typedef float f32x2 __attribute__((ext_vector_type(2)));
typedef __bf16 bf16x2 __attribute__((ext_vector_type(2)));
typedef unsigned u32x4 __attribute__((ext_vector_type(4)));
typedef unsigned u32x2 __attribute__((ext_vector_type(2)));
typedef int i32x4 __attribute__((ext_vector_type(4)));
#define LAS __attribute__((address_space(3)))
#define DEV __device__ __forceinline__
typedef __amdgpu_buffer_rsrc_t rsrc_t;

constexpr int S = 2048, D = 640, F = 1536;
constexpr unsigned RW_PITCH = 4096 + 128;
constexpr unsigned OFF_E = 0, OFF_FL = 4096, FL_REP = 33024, FL_STR = 32;
constexpr unsigned FK0 = 0, FW0 = 64, F10 = 320, F20 = 512;
constexpr unsigned OFF_RW = 1u << 20, OFF_W2T = 4u << 20, OFF_H = 6u << 20, OFF_Z = 13u << 20, OFF_G = 16u << 20, WS_USED = 17u << 20;
constexpr int LDS_BYTES = 160 * 1024;
constexpr unsigned L_CONV = 98304, CONV_CH = 17600, L_XCH = 133504, L_TO = 149888, L_ZI = 65536;
constexpr int AUX_SC1 = 16;
constexpr unsigned OOB = 0x80000000u, SPIN_CAP = 1u << 16;
constexpr unsigned CODE_BYTES = 28 * 1024;

struct Params {
  const u16 *q, *k, *v, *sk, *fk, *ssd, *w1, *b1, *w2, *b2, *gw;
  u16* out;
  char* ws;
};

DEV f32x4 mfma(u32x4 a, u32x4 b, f32x4 c) { return __builtin_amdgcn_mfma_f32_16x16x32_bf16(__builtin_bit_cast(s16x8, a), __builtin_bit_cast(s16x8, b), c, 0, 0, 0); }
DEV i32x4 mksrd(const void* p, unsigned bytes) {
  unsigned long long a = (unsigned long long)p;
  i32x4 r; r.x = (int)(unsigned)a; r.y = (int)(unsigned)(a >> 32); r.z = (int)bytes; r.w = 0x00020000; return r;
}
DEV rsrc_t mkrsrc(const void* p, unsigned bytes) { return __builtin_amdgcn_make_buffer_rsrc((void*)p, 0, (int)bytes, 0x00020000); }
template <int POL, int IMM> DEV void dma16(i32x4 srd, unsigned voff, unsigned soff, unsigned lds) {
  if (POL == 0) asm volatile("s_mov_b32 m0, %2\n\ts_nop 0\n\tbuffer_load_dwordx4 %0, %1, %3 offen offset:%4 lds" :: "v"(voff), "s"(srd), "s"(lds), "s"(soff), "i"(IMM) : "memory");
  if (POL == 1) asm volatile("s_mov_b32 m0, %2\n\ts_nop 0\n\tbuffer_load_dwordx4 %0, %1, %3 offen offset:%4 lds" :: "v"(voff), "s"(srd), "s"(lds), "s"(soff), "i"(IMM) : "memory");
  if (POL == 2) asm volatile("s_mov_b32 m0, %2\n\ts_nop 0\n\tbuffer_load_dwordx4 %0, %1, %3 offen offset:%4 sc1 lds" :: "v"(voff), "s"(srd), "s"(lds), "s"(soff), "i"(IMM) : "memory");
}
template <int POL> DEV void dma16n(i32x4 srd, unsigned voff, unsigned soff, unsigned lds, int n) {
  if (n == 0) dma16<POL, 0>(srd, voff, soff, lds);
  if (n == 1) dma16<POL, 1024>(srd, voff, soff, lds);
  if (n == 2) dma16<POL, 2048>(srd, voff, soff, lds);
  if (n == 3) dma16<POL, 3072>(srd, voff, soff, lds);
}
DEV void setm0(unsigned lds) { asm volatile("s_mov_b32 m0, %0" :: "s"(lds) : "memory"); }
template <int IMM> DEV void dma16m(i32x4 srd, unsigned voff, unsigned soff) {
  asm volatile("buffer_load_dwordx4 %0, %1, %2 offen offset:%3 lds" :: "v"(voff), "s"(srd), "s"(soff), "i"(IMM) : "memory");
}
template <int N> DEV void waitvm() { asm volatile("s_waitcnt vmcnt(%0)" :: "i"(N) : "memory"); }
DEV void waitlgkm0() { asm volatile("s_waitcnt lgkmcnt(0)" ::: "memory"); }
DEV void bar() { asm volatile("s_barrier" ::: "memory"); }
DEV void sched0() { __builtin_amdgcn_sched_barrier(0); }
DEV unsigned opq(unsigned x) { asm volatile("" : "+v"(x)); return x; }
DEV unsigned opqs(unsigned x) { asm volatile("" : "+s"(x)); return x; }
DEV u32x4 lds128(unsigned a) { return *(LAS u32x4*)a; }
DEV u32x2 lds64(unsigned a) { return *(LAS u32x2*)a; }
DEV u32x2 ldstr(unsigned a) { return __builtin_bit_cast(u32x2, __builtin_amdgcn_ds_read_tr16_b64_v4i16((LAS s16x4*)a)); }
DEV unsigned pk2(float lo, float hi) { f32x2 z = {lo, hi}; return __builtin_bit_cast(unsigned, __builtin_convertvector(z, bf16x2)); }
DEV float blo(unsigned w) { return __builtin_bit_cast(float, w << 16); }
DEV float bhi(unsigned w) { return __builtin_bit_cast(float, w & 0xffff0000u); }
DEV float dot2(unsigned a, unsigned b, float c) { return __builtin_amdgcn_fdot2_f32_bf16(__builtin_bit_cast(bf16x2, a), __builtin_bit_cast(bf16x2, b), c, false); }
DEV void st128(u32x4 v, rsrc_t r, unsigned voff, unsigned soff) { __builtin_amdgcn_raw_buffer_store_b128(v, r, voff, soff, AUX_SC1); sched0(); asm volatile("s_nop 1"); }

#define TS(idx) do {} while (0)
#define TSDECL
#define TSARG
#define TSPASS
#define TSFLUSH(lo, hi)

DEV u32x2 flag_load(rsrc_t rws, unsigned voff) { return __builtin_amdgcn_raw_buffer_load_b64(rws, voff, OFF_FL, AUX_SC1); }
DEV void flag_wait(rsrc_t rws, unsigned voff, u32x2 first, u32x2 tag) {
  bool bad = (int)voff >= 0 && (first.x != tag.x || first.y != tag.y);
  if (__builtin_expect(__builtin_amdgcn_ballot_w64(bad) != 0ull, 0)) {
    unsigned spins = 0;
#pragma unroll 1
    do {
      __builtin_amdgcn_s_sleep(2);
      asm volatile("" ::: "memory");
      const u32x2 v = flag_load(rws, bad ? voff : OOB);
      bad = bad && (v.x != tag.x || v.y != tag.y);
    } while (__builtin_amdgcn_ballot_w64(bad) != 0ull && ++spins < SPIN_CAP);
  }
  asm volatile("" ::: "memory");
}
DEV void flag_store(rsrc_t rws, unsigned vrep, unsigned idx, u32x2 tag) { __builtin_amdgcn_raw_buffer_store_b64(tag, rws, vrep, OFF_FL + idx * FL_STR, AUX_SC1); }

struct Ctx {
  rsrc_t rws; i32x4 sws;
  int w, lane;
  unsigned l16;
  unsigned vrep;
  unsigned pk, pw, p1, p2;
  unsigned wbase, hbase;
};
DEV u32x4 load_w(const Ctx& c, int n) { return __builtin_amdgcn_raw_buffer_load_b128(c.rws, c.l16 + (n & 3) * 1024, c.wbase + (n >> 2) * 4096, AUX_SC1); }
DEV u32x4 load_h(const Ctx& c, int n) {
  return __builtin_amdgcn_raw_buffer_load_b128(c.rws, c.l16 + (n & 3) * 1024, c.hbase + (((c.w + 1 + n / 12) & 3) * 48 + ((n % 12) & ~3)) * 1024, AUX_SC1);
}

DEV void conv_pads(unsigned lbase, int w, int lane) {
  const unsigned cbase = lbase + (w >> 1) * CONV_CH, half = w & 1;
  const unsigned rb = cbase + half * 4576, fb = cbase + 9152 + half * 4224;
  const int l = lane & 31;
  *(LAS u32x4*)(l < 28 ? rb + 4096 + l * 16 : fb + (l - 28) * 16) = u32x4{0u, 0u, 0u, 0u};
}
DEV void conv_load_f(const Params& p, const Ctx& c, int c0, unsigned lbase, int n) {
  const int half = c.w & 1;
  dma16n<1>(mksrd(p.fk, D * S * 2), c.l16, (c0 + (c.w >> 1)) * 4096, lbase + (c.w >> 1) * CONV_CH + 9152 + 60 + half * 4228, n);
}
DEV void conv_load_r(const Ctx& c, int c0, unsigned lbase, int n) {
  const int half = c.w & 1;
  dma16n<2>(c.sws, c.l16, OFF_RW + (c0 + (c.w >> 1)) * RW_PITCH + 2 - 2 * half, lbase + (c.w >> 1) * CONV_CH + half * 4576, n);
}

DEV void phase_kv(const Params& p, const Ctx& c, int kh TSARG) {
  const int w = c.w, lane = c.lane;
  const i32x4 sk_ = mksrd(p.k, S * D * 2), sv_ = mksrd(p.v, S * D * 2);
  const int qd = (kh * 26) >> 8, cs = kh - qd * 10, s0 = qd * 512;
  const int g = lane >> 4, q4 = (lane >> 2) & 3, p4 = lane & 3, i16 = lane & 15;
  constexpr unsigned L_V = 66560;
  const unsigned vrow = (lane >> 3) * 1280 + (((lane & 7) * 16) ^ ((g & 1) * 32)) + cs * 128;
  const unsigned vrA = vrow ^ ((w & 1) * 64), vrB = (vrA ^ 64) + 8 * 1280 - 1024, vrC = vrA + 8 * 1280 - 1024;
  auto slab = [&](int j) {
    const unsigned so = (s0 + j * 64 + w * 16) * 1280;
    dma16<1, 0>(sk_, vrA, so, j * 8192 + 1024 + w * 2048);
    dma16<1, 1024>(sk_, vrB, so, j * 8192 + 1024 + w * 2048);
    dma16<1, 0>(sv_, vrA, so, L_V + j * 8192 + w * 2048);
    dma16<1, 1024>(sv_, vrC, so, L_V + j * 8192 + w * 2048);
  };
  dma16<1, 0>(sk_, vrow + (unsigned)((s0 - 8) * 1280), 0, 0);
  u32x2 tap;
  asm volatile("global_load_dwordx2 %0, %1, off" : "=v"(tap) : "v"(p.sk + (cs * 64 + w * 16 + i16) * 4) : "memory");
  slab(0); slab(1);
  sched0();
  unsigned ka[8];
#pragma unroll
  for (int x = 0; x < 8; ++x) {
    const unsigned row = 8 + 16 * (i16 >> 2) + 4 * (x >> 1) + (i16 & 3) - 2 * (x & 1) - (g >> 1);
    ka[x] = opq(row * 128 + ((w ^ (((row >> 1) & 1) | (((row >> 4) & 1) << 1))) * 32) + (g & 1) * 16);
  }
  unsigned msk[4];
#pragma unroll
  for (int d = 0; d < 4; ++d) msk[d] = opq(((i16 >> 3) == (g & 1) && ((i16 & 7) >> 1) == d) ? (0xffffu << (16 * (i16 & 1))) : 0u);
  const unsigned sel = opq((g >> 1) ? 0x03020302u : 0x01000100u);
  const unsigned va = opq(L_V + (16 * g + q4) * 128 + (w ^ ((q4 >> 1) | ((g & 1) << 1))) * 32 + p4 * 8);
  const unsigned ta = opq(L_TO + w * 2304 + i16 * 144 + (96 - 32 * g));
  const unsigned ra = opq(L_TO + w * 2304 + (lane >> 3) * 144 + (lane & 7) * 16);
  const unsigned vo = opq((lane >> 3) * RW_PITCH + (lane & 7) * 16), vo2 = opq(vo + 8 * RW_PITCH);
  const unsigned so0 = OFF_RW + (cs * 64 + w * 16) * RW_PITCH + (1984 - s0 - 448) * 2;
  sched0();
  slab(2);
  u32x4 K[2][8]; u32x2 V[2][4]; u32x4 O[16];
  auto rd = [&](int j, int x) {
    if (x < 8) K[j & 1][x] = lds128(ka[x] + j * 8192); else V[j & 1][x - 8] = ldstr(va + j * 8192 + (x - 8) * 512);
  };
  auto put = [&](int n) { __builtin_amdgcn_raw_buffer_store_b128(O[n], c.rws, ((n & 1) ? vo2 : vo) + (7 - (n >> 1)) * 128, so0, AUX_SC1); };
  TS(1);
  waitvm<8>(); bar();
  TS(2);
#pragma unroll
  for (int x = 0; x < 12; ++x) rd(0, x);
  asm volatile("" : "+v"(tap));
  const unsigned r01 = __builtin_amdgcn_perm(tap.x, tap.x, sel), r23 = __builtin_amdgcn_perm(tap.y, tap.y, sel);
  const u32x4 B0 = {r01 & msk[0], r01 & msk[1], r01 & msk[2], r01 & msk[3]}, B1 = {r23 & msk[0], r23 & msk[1], r23 & msk[2], r23 & msk[3]};
  slab(3);
  sched0();
#pragma unroll
  for (int j = 0; j < 8; ++j) {
    if (j > 0) { O[2 * j - 2] = lds128(ra); O[2 * j - 1] = lds128(ra + 8 * 144); }
    sched0();
    if (j < 7) {
      if (j <= 4) waitvm<8>(); else if (j == 5) waitvm<8>(); else waitvm<8>();
      bar();
    }
    f32x4 ks[4]; f32x2 vf[8];
#pragma unroll
    for (int x = 0; x < 8; ++x) {
      const int t = x & 3;
      ks[t] = x < 4 ? mfma(K[j & 1][2 * t], B0, f32x4{0.f, 0.f, 0.f, 0.f}) : mfma(K[j & 1][2 * t + 1], B1, ks[t]);
      sched0();
      if (j < 7) rd(j + 1, x < 4 ? 2 * x : 2 * (x - 4) + 1);
      const unsigned vv = (x & 1) ? V[j & 1][x >> 1].y : V[j & 1][x >> 1].x;
      vf[x] = f32x2{blo(vv), bhi(vv)};
      asm volatile("" : "+v"(vf[x]));
      sched0();
    }
    asm volatile("" :: "v"(K[j & 1][0]), "v"(K[j & 1][1]), "v"(K[j & 1][2]), "v"(K[j & 1][3]), "v"(K[j & 1][4]), "v"(K[j & 1][5]), "v"(K[j & 1][6]), "v"(K[j & 1][7]));
    if (j < 7) { rd(j + 1, 8); rd(j + 1, 9); rd(j + 1, 10); rd(j + 1, 11); }
    sched0();
    asm volatile("" : "+a"(ks[0]), "+a"(ks[1]), "+a"(ks[2]), "+a"(ks[3]));
    f32x2 pr[8];
#pragma unroll
    for (int x = 0; x < 8; ++x) { pr[x] = f32x2{ks[x >> 1][(x & 1) * 2], ks[x >> 1][(x & 1) * 2 + 1]}; asm volatile("" : "+v"(pr[x])); }
    sched0();
    unsigned r[8];
#pragma unroll
    for (int x = 0; x < 8; ++x) pr[x] *= vf[x];
    sched0();
#pragma unroll
    for (int x = 0; x < 8; ++x) asm volatile("v_cvt_pk_bf16_f32 %0, %1, %2" : "=v"(r[7 - x]) : "v"(pr[x].y), "v"(pr[x].x));
    *(LAS u32x4*)ta = u32x4{r[0], r[1], r[2], r[3]}; *(LAS u32x4*)(ta + 16) = u32x4{r[4], r[5], r[6], r[7]};
    sched0();
    if (j < 4) slab(j + 4);
    if (j >= 4 && j < 7) { put(4 * (j - 4)); put(4 * (j - 4) + 1); put(4 * (j - 4) + 2); put(4 * (j - 4) + 3); }
    if (j == 7) { put(12); put(13); }
    sched0();
  }
  O[14] = lds128(ra); O[15] = lds128(ra + 8 * 144);
  put(14); put(15);
  __builtin_amdgcn_raw_buffer_store_b128(u32x4{0u, 0u, 0u, 0u}, c.rws, (qd == 0 && lane < 16) ? lane * RW_PITCH + 4096 : OOB, OFF_RW + (cs * 64 + w * 16) * RW_PITCH, AUX_SC1);
}

DEV void phase_w2(const Params& p, const Ctx& c, int wh TSARG) {
  const int w = c.w, lane = c.lane, g = lane >> 4, q4 = (lane >> 2) & 3, p4 = lane & 3;
  const i32x4 sw2 = mksrd(p.w2, F * D * 2);
#pragma unroll
  for (int n = 0; n < 20; ++n) {
    const unsigned r7 = ((4 * (n % 10) + g) * 13) >> 6;
    dma16<1, 0>(sw2, c.l16 ^ (r7 * 32), wh * 81920 + (n / 10) * 40960 + (w * 10 + n % 10) * 1024, (n / 10) * 40960 + (w * 10 + n % 10) * 1024);
    if (n == 9) {
      sched0();
    }
  }
  unsigned wa[10];
#pragma unroll
  for (int n = 0; n < 10; ++n) {
    const int T = n % 5; const unsigned row = 4 * g + q4;
    const unsigned col = (w * 2 + n / 5) * 80 + (T < 4 ? 32 * (T >> 1) + 8 * p4 + 4 * (T & 1) : 64 + 4 * p4);
    wa[n] = opq(row * 1280 + ((col * 2) ^ ((row & 7) * 32)));
  }
  TS(1);
#pragma unroll
  for (int h = 0; h < 2; ++h) {
    waitvm<10>(); bar();
    TS(2 + h);
    u32x2 lo[10], hi[10];
#pragma unroll
    for (int n = 0; n < 10; ++n) { lo[n] = ldstr(wa[n] + h * 40960); hi[n] = ldstr(wa[n] + h * 40960 + 16 * 1280); }
#pragma unroll
    for (int n = 0; n < 10; ++n)
      __builtin_amdgcn_raw_buffer_store_b128(u32x4{lo[n].x, lo[n].y, hi[n].x, hi[n].y}, c.rws, c.l16, OFF_W2T + (((w * 2 + n / 5) * 48 + wh * 2 + h) * 5 + n % 5) * 1024, AUX_SC1);
    sched0();
  }
}

template <int W0, int NW, bool LAST>
DEV void conv_compute(const Ctx& c, unsigned lbase, unsigned dbits_in, u32x4 (&W)[60], u32x4 (&H)[48], u32x2 tag, unsigned pub, f32x4 (&keep)[4], rsrc_t rq, unsigned gq TSARG) {
  const int w = c.w, lane = c.lane, half = w & 1, g = lane >> 4, n16 = lane & 15;
  const unsigned cbase = lbase + (w >> 1) * CONV_CH;
  const unsigned ua = opq(cbase + (n16 & 1) * 4576 + 448 - 64 * (n16 >> 1) + 16 * g + 256 * half);
  const unsigned fa = opq(cbase + 9152 + (n16 & 1) * 4228 + 4 * n16 + 16 * g + 256 * half);
  const unsigned fa2 = opq(fa + 8);
  const unsigned xw = opq(L_XCH + w * 4096 + c.l16);
  u32x4 U[2][8]; u32x2 Fl[2][8], Fh[2][8];
  f32x4 acc[8];
  u32x2 poll;
#pragma unroll
  for (int j = 0; j < 8; ++j) { U[0][j] = lds128(ua + 512 * (7 - j)); sched0(); }
  const unsigned dbits = half ? 0u : dbits_in;
  const int dsel = 15 - n16 - 4 * g;
  const u32x4 Df = {dsel == 0 ? dbits : 0u, dsel == 1 ? dbits : 0u, dsel == 2 ? dbits : 0u, dsel == 3 ? dbits : 0u};
  sched0();
#pragma unroll
  for (int j = 0; j < 8; ++j) {
    Fl[0][j] = lds64(fa + 512 * j); Fh[0][j] = lds64(fa2 + 512 * j); sched0();
    acc[j] = mfma(U[0][j], Df, f32x4{0.f, 0.f, 0.f, 0.f}); sched0();
  }
  int wn = 0;
#pragma unroll
  for (int r = 0; r < 3; ++r) {
    const int cur = r & 1, nxt = cur ^ 1;
    int m = 0;
#pragma unroll
    for (int E = 0; E < 8; ++E)
#pragma unroll
      for (int I = E; I < 8; ++I) {
        acc[I] = mfma(U[cur][I - E], u32x4{Fl[cur][E].x, Fl[cur][E].y, Fh[cur][E].x, Fh[cur][E].y}, acc[I]);
        sched0();
        if (m < 32 && (m & 3) != 3) {
          const int x = (m >> 2) * 3 + (m & 3);
          if (x % 3 == 0) U[nxt][x / 3] = lds128(ua + 512 * (7 - x / 3) + 64 * (r + 1));
          if (x % 3 == 1) Fl[nxt][x / 3] = lds64(fa + 512 * (x / 3) + 64 * (r + 1));
          if (x % 3 == 2) Fh[nxt][x / 3] = lds64(fa2 + 512 * (x / 3) + 64 * (r + 1));
          sched0();
        }
        if ((m & 3) == 3 && wn < NW) { W[W0 + wn] = load_w(c, W0 + wn); ++wn; sched0(); }
        if (LAST && r == 2 && m == 21) { poll = flag_load(c.rws, c.p1); sched0(); }
        ++m;
      }
    if (LAST && r == 0) { waitvm<9>(); bar(); flag_store(c.rws, c.vrep, pub, tag); sched0(); }
  }
  if (LAST) { TS(14); flag_wait(c.rws, c.p1, poll, tag); bar(); TS(15); }
  auto extra = [&](int m) {
    if (LAST) { if (m % 3 == 2) { const int x = m / 3; if (x & 1) W[W0 + NW + (x >> 1)] = load_w(c, W0 + NW + (x >> 1)); else H[x >> 1] = load_h(c, x >> 1); sched0(); } }
    else if (m % 3 == 2 && m < 30) { H[m / 3] = __builtin_amdgcn_raw_buffer_load_b128(rq, gq + ((m / 3) % 5) * 256, (m / 15) * 4 * D * 2, 0); sched0(); }
  };
  if (half) {
    int m = 0;
#pragma unroll
    for (int E = 0; E < 8; ++E)
#pragma unroll
      for (int I = E; I < 8; ++I) {
        acc[I] = mfma(U[1][I - E], u32x4{Fl[1][E].x, Fl[1][E].y, Fh[1][E].x, Fh[1][E].y}, acc[I]);
        sched0(); extra(m);
        if (I == 7 && E < 4) { *(LAS f32x4*)(xw + E * 1024) = acc[E]; sched0(); }
        ++m;
      }
#pragma unroll
    for (int i = 0; i < 4; ++i) { keep[i] = acc[4 + i]; asm volatile("" : "+v"(keep[i])); }
  } else {
    int m = 0;
#pragma unroll
    for (int P = 0; P < 2; ++P)
#pragma unroll
      for (int E = 0; E < 8; ++E)
#pragma unroll
        for (int I = 4 - 4 * P; I < 8 - 4 * P; ++I)
          if (I >= E) {
            acc[I] = mfma(U[1][I - E], u32x4{Fl[1][E].x, Fl[1][E].y, Fh[1][E].x, Fh[1][E].y}, acc[I]);
            sched0(); extra(m);
            if (m == 25 || m == 27 || m == 28 || m == 30) { const int i = m == 25 ? 0 : m == 27 ? 1 : m == 28 ? 2 : 3; *(LAS f32x4*)(xw + i * 1024) = acc[4 + i]; sched0(); }
            ++m;
          }
#pragma unroll
    for (int i = 0; i < 4; ++i) { keep[i] = acc[i]; asm volatile("" : "+v"(keep[i])); }
  }
  waitlgkm0(); bar();
}

DEV void phase_g1(const Params& p, const Ctx& c, int rt, int ct, int off, int xs, int gi, u32x4 (&W)[60], u32x2& tag TSARG) {
  const int w = c.w, lane = c.lane, c0 = gi * 2;
  const i32x4 sx = mksrd(p.q, S * D * 2), sw = mksrd(p.w1, D * F * 2);
  const int g = lane >> 4, q4 = (lane >> 2) & 3, p4 = lane & 3, n16 = lane & 15, wf = w & 1, wsn = w >> 1;
  unsigned vx[4], vw[4];
#pragma unroll
  for (int j = 0; j < 4; ++j) {
    vx[j] = (rt * 128 + w * 32 + (lane >> 3)) * 1280 + ((lane & 7) ^ ((g << 1) | (w & 1))) * 16 + j * (8 * 1280 - 1024);
    vw[j] = (((w * 16 + g) * 3072 + ct * 256 + (n16 ^ (g << 1)) * 16) ^ ((j >> 1) * 128)) + j * (4 * 3072 - 1024);
  }
  auto kstage = [&](int st) {
    int ks = (st & ~3) + ((st + off) & (st < 8 ? 3 : 1)) + xs; if (ks >= 10) ks -= 10; return ks;
  };
  unsigned sox = 0, sow = 0;
  auto prep = [&](int st, int op) {
    if (op == 0) sox = opqs(kstage(st) * 128); else sow = opqs(kstage(st) * 64 * 3072);
    setm0((st % 3) * 32768 + w * 4096 + op * 16384);
  };
  auto issue = [&](int j) {
    if (j == 0) dma16m<0>(sx, vx[0], sox);
    if (j == 1) dma16m<1024>(sx, vx[1], sox);
    if (j == 2) dma16m<2048>(sx, vx[2], sox);
    if (j == 3) dma16m<3072>(sx, vx[3], sox);
    if (j == 4) dma16m<0>(sw, vw[0], sow);
    if (j == 5) dma16m<1024>(sw, vw[1], sow);
    if (j == 6) dma16m<2048>(sw, vw[2], sow);
    if (j == 7) dma16m<3072>(sw, vw[3], sow);
  };
  auto stage_all = [&](int st) {
    prep(st, 0); asm volatile("s_nop 0"); issue(0); issue(1); issue(2); issue(3);
    prep(st, 1); asm volatile("s_nop 0"); issue(4); issue(5); issue(6); issue(7);
  };
  stage_all(0);
  u32x2 bias[4];
#pragma unroll
  for (int a = 0; a < 4; ++a) asm volatile("global_load_dwordx2 %0, %1, off offset:%2" : "=v"(bias[a]) : "v"(p.b1 + ct * 128 + wf * 64 + g * 4), "i"(a * 32) : "memory");
  sched0();
  conv_pads(L_CONV, w, lane);
  unsigned wa[2][4], xa[2][2];
#pragma unroll
  for (int a = 0; a < 4; ++a) {
    wa[0][a] = opq(16384 + (8 * g + q4) * 256 + p4 * 8 + ((((wf ^ (g & 1)) << 2) | (a ^ q4)) * 32));
    wa[1][a] = opq(wa[0][a] + 65536);
  }
  xa[0][0] = opq((wsn * 64 + 32 * ((n16 >> 1) & 1) + 2 * (n16 >> 2) + (n16 & 1)) * 128 + ((g ^ (n16 >> 1)) * 16)); xa[0][1] = opq(xa[0][0] ^ 64);
  xa[1][0] = opq(xa[0][0] + 65536); xa[1][1] = opq(xa[0][1] + 65536);
  sched0();
  stage_all(1);
  asm volatile("buffer_load_dwordx2 %0, off, %1, 0 sc1" : "=v"(tag) : "s"(c.sws) : "memory");
  f32x4 acc[4][4];
  u32x2 Al[2][4], Ah[2][4]; u32x4 B[2][4];
  auto rd = [&](int buf, int st, int kk, int x) {
    const int sl = st % 3, hi = sl == 2;
    const unsigned base = (sl & 1) * 32768;
    constexpr int kind[12] = {1, 2, 0, 0, 1, 0, 2, 0, 1, 2, 1, 2}, idx[12] = {0, 0, 0, 1, 1, 2, 1, 3, 2, 2, 3, 3};
    if (kind[x] == 0) B[buf][idx[x]] = lds128(xa[hi][kk] + base + idx[x] * 1024);
    if (kind[x] == 1) Al[buf][idx[x]] = ldstr(wa[hi][idx[x]] + base + kk * 8192);
    if (kind[x] == 2) Ah[buf][idx[x]] = ldstr(wa[hi][idx[x]] + base + kk * 8192 + 1024);
  };
  waitvm<9>(); bar();
  TS(1);
#pragma unroll
  for (int x = 0; x < 12; ++x) rd(0, 0, 0, x);
#pragma unroll
  for (int a = 0; a < 4; ++a) {
    asm volatile("" : "+v"(bias[a]));
    const f32x4 bv = {blo(bias[a].x), bhi(bias[a].x), blo(bias[a].y), bhi(bias[a].y)};
#pragma unroll
    for (int b = 0; b < 4; ++b) acc[a][b] = bv;
  }
  sched0();
  u32x2 pollk, pollw;
  int wn = 0;
#pragma unroll
  for (int st = 0; st < 10; ++st) {
#pragma unroll
    for (int m = 0; m < 16; ++m) {
      const int a = m >> 2, b = m & 3;
      acc[a][b] = mfma(u32x4{Al[0][a].x, Al[0][a].y, Ah[0][a].x, Ah[0][a].y}, B[0][b], acc[a][b]);
      sched0();
      if (m < 12) { rd(1, st, 1, m); sched0(); }
      if (st == 0) {
        if (m == 0 || m == 8) { prep(2, m >> 3); sched0(); }
        if (m & 1) { issue(m >> 1); sched0(); }
      } else if (st + 2 < 10) {
        if (m == 0) { prep(st + 2, 1); sched0(); }
        if ((m & 3) == 1) { issue(4 + (m >> 2)); sched0(); }
      }
      if (st >= 8 && (m & 3) == 1) { W[wn] = load_w(c, wn); ++wn; sched0(); }
      if (st == 9 && m == 14) { pollk = flag_load(c.rws, c.pk); sched0(); }
    }
    if (st < 9) {
      waitlgkm0();
      waitvm<8>();
      if (st == 0) asm volatile("v_add_u32 %0, 1, %0" : "+v"(tag.x), "+v"(tag.y));
      if (st == 7) { asm volatile("" : "+v"(pollw)); flag_wait(c.rws, c.pw, pollw, tag); }
      bar();
      if (st == 6) asm volatile("buffer_load_dwordx2 %0, %1, %2, %3 offen sc1" : "=v"(pollw) : "v"(c.pw), "s"(c.sws), "s"(OFF_FL) : "memory");
    }
#pragma unroll
    for (int m = 0; m < 16; ++m) {
      const int a = m >> 2, b = m & 3;
      acc[a][b] = mfma(u32x4{Al[1][a].x, Al[1][a].y, Ah[1][a].x, Ah[1][a].y}, B[1][b], acc[a][b]);
      if (st < 9) {
        sched0();
        if (m < 12) { rd(0, st + 1, 0, m); sched0(); }
        if (st + 3 < 10) {
          if (m == 0) { prep(st + 3, 0); sched0(); }
          if ((m & 3) == 1) { issue(m >> 2); sched0(); }
        }
        if (st == 7 && (m & 3) == 1) { conv_load_f(p, c, c0, L_CONV, m >> 2); sched0(); }
        if (st == 8 && (m & 3) == 1) { W[wn] = load_w(c, wn); ++wn; sched0(); }
      } else {
        sched0();
        if ((m & 3) == 1) { W[wn] = load_w(c, wn); ++wn; sched0(); }
      }
    }
  }
#pragma unroll
  for (int a = 0; a < 4; ++a) asm volatile("" : "+a"(acc[a][0]), "+a"(acc[a][1]), "+a"(acc[a][2]), "+a"(acc[a][3]));
  TS(11);
  flag_wait(c.rws, c.pk, pollk, tag); bar();
  TS(12);
  const unsigned ho = OFF_H + ((rt * 8 + wsn * 4) * 48 + ct * 4 + wf * 2) * 1024;
#pragma unroll
  for (int n = 0; n < 8; ++n) {
    const int b = n >> 1, P = n & 1;
    constexpr float c0f = -2.f * 0.7978845608028654f * 1.4426950408889634f, c1f = c0f * 0.044715f;
    f32x2 z[4], t[4];
    asm volatile("" : "+a"(acc[2 * P][b]), "+a"(acc[2 * P + 1][b]));
    z[0] = f32x2{acc[2 * P][b][0], acc[2 * P][b][1]}; z[1] = f32x2{acc[2 * P][b][2], acc[2 * P][b][3]};
    z[2] = f32x2{acc[2 * P + 1][b][0], acc[2 * P + 1][b][1]}; z[3] = f32x2{acc[2 * P + 1][b][2], acc[2 * P + 1][b][3]};
#pragma unroll
    for (int x = 0; x < 4; ++x) asm volatile("" : "+v"(z[x]));
    sched0();
#pragma unroll
    for (int x = 0; x < 4; ++x) t[x] = z[x] * z[x];
    sched0();
#pragma unroll
    for (int x = 0; x < 4; ++x) t[x] = __builtin_elementwise_fma(t[x], f32x2{c1f, c1f}, f32x2{c0f, c0f});
    sched0();
#pragma unroll
    for (int x = 0; x < 4; ++x) t[x] = t[x] * z[x];
    sched0();
#pragma unroll
    for (int x = 0; x < 4; ++x) t[x] = f32x2{__builtin_amdgcn_exp2f(t[x].x), __builtin_amdgcn_exp2f(t[x].y)};
    sched0();
    if (n < 4) conv_load_r(c, c0, L_CONV, n); else W[16 + n - 4] = load_w(c, 16 + n - 4);
    sched0();
#pragma unroll
    for (int x = 0; x < 4; ++x) t[x] = t[x] + f32x2{1.f, 1.f};
    sched0();
#pragma unroll
    for (int x = 0; x < 4; ++x) t[x] = f32x2{__builtin_amdgcn_rcpf(t[x].x), __builtin_amdgcn_rcpf(t[x].y)};
    sched0();
#pragma unroll
    for (int x = 0; x < 4; ++x) t[x] = t[x] * z[x];
    sched0();
    const u32x4 o = {pk2(t[0].x, t[0].y), pk2(t[1].x, t[1].y), pk2(t[2].x, t[2].y), pk2(t[3].x, t[3].y)};
    st128(o, c.rws, c.l16, ho + (b * 48 + P) * 1024);
    sched0();
  }
  waitvm<9>(); bar();
}

__global__ __launch_bounds__(256, 1) void k_fused(Params p) {
  extern __shared__ __attribute__((aligned(16))) char smem[];
  const int b = blockIdx.x, xcd = b & 7, i = b >> 3;
  const int w = __builtin_amdgcn_readfirstlane(threadIdx.x >> 6), lane = threadIdx.x & 63;
  TSDECL; TS(0);
  unsigned pf;
  {
    unsigned long long pc; asm volatile("s_getpc_b64 %0" : "=s"(pc));
    const i32x4 sc = mksrd((const void*)(pc & ~127ull), CODE_BYTES);
    asm volatile("buffer_load_dword %0, %1, %2, 0 offen" : "=v"(pf) : "v"(threadIdx.x < 8 ? (unsigned)(i * 1024 + threadIdx.x * 128) : OOB), "s"(sc) : "memory");
  }
  const bool helper = b >= 192;
  const int ri = i >> 3, cb = i & 7, R = xcd * 4 + ri;
  const int pairB = helper ? 2 * b - 191 : b, c0 = pairB * 2;
  const unsigned lbase = helper ? 0u : L_CONV;
  const rsrc_t rsd = mkrsrc(p.ssd, D * 2), rq = mkrsrc(p.q, S * D * 2);
  Ctx c;
  c.rws = mkrsrc(p.ws, WS_USED); c.sws = mksrd(p.ws, WS_USED); c.w = w; c.lane = lane; c.l16 = lane * 16;
  c.wbase = OFF_W2T + (cb * 48 + w * 12) * 5120; c.hbase = OFF_H + (R * 4 * 48 + w * 12) * 1024;
  c.vrep = threadIdx.x < 8 ? threadIdx.x * FL_REP : OOB;
  const unsigned prep_ = xcd * FL_REP;
  c.pk = threadIdx.x < 4 ? prep_ + (FK0 + (pairB >> 5) + lane * 10) * FL_STR : OOB;
  c.pw = threadIdx.x < 24 ? prep_ + (FW0 + lane) * FL_STR : OOB;
  c.p1 = threadIdx.x < 12 ? prep_ + (F10 + (R >> 1) * 12 + lane) * FL_STR : OOB;
  c.p2 = threadIdx.x < 42 ? prep_ + (F20 + (lane < 40 ? cb * 40 + lane : 192 + 2 * ((2 * ri + (lane & 1)) * 8 + xcd))) * FL_STR : OOB;
  u32x2 tag;
  u32x4 W[60], H[48];
  f32x4 keep[4];
  unsigned dbits;
  const int r6 = (i * 43) >> 8, c6 = i - r6 * 6;
  const int rt = (xcd >> 1) * 4 + r6, ct = (xcd & 1) * 6 + c6;
  const unsigned pub = helper ? F20 + pairB - 1 : F10 + rt * 12 + ct;
  const unsigned xr = L_XCH + (w ^ 1) * 4096 + lane * 16;
  if (helper) {
    const int hz = b - 192, cA = c0 - 2;
    tag = __builtin_amdgcn_raw_buffer_load_b64(c.rws, 0, OFF_E, AUX_SC1);
    const unsigned dA = __builtin_amdgcn_raw_buffer_load_b16(rsd, 0, (cA + (w >> 1)) * 2, 0);
    dbits = __builtin_amdgcn_raw_buffer_load_b16(rsd, 0, (c0 + (w >> 1)) * 2, 0);
    sched0();
    if (hz < 40) phase_kv(p, c, hz TSPASS); else phase_w2(p, c, hz - 40 TSPASS);
    TS(9);
    waitlgkm0(); waitvm<0>(); bar();
    asm volatile("" :: "v"(pf));
    tag.x = opq(tag.x) + 1u;
    flag_store(c.rws, c.vrep, hz < 40 ? FK0 + hz : FW0 + hz - 40, tag);
    TS(10);
    conv_pads(0, w, lane); conv_pads(L_CONV, w, lane);
    flag_wait(c.rws, c.pw, flag_load(c.rws, c.pw), tag);
    waitlgkm0(); bar();
    TS(11);
#pragma unroll
    for (int n = 0; n < 4; ++n) conv_load_f(p, c, cA, L_CONV, n);
#pragma unroll
    for (int n = 0; n < 4; ++n) conv_load_f(p, c, c0, 0, n);
#pragma unroll
    for (int n = 0; n < 4; ++n) W[n] = load_w(c, n);
    sched0();
    flag_wait(c.rws, c.pk, flag_load(c.rws, c.pk), tag);
    bar();
    TS(12);
#pragma unroll
    for (int n = 0; n < 4; ++n) conv_load_r(c, cA, L_CONV, n);
#pragma unroll
    for (int n = 0; n < 4; ++n) conv_load_r(c, c0, 0, n);
    sched0();
    const int g = lane >> 4, i16 = lane & 15, row0 = xcd * 256 + (i - 24) * 32 + w * 8 + g;
    u32x4 gw[5];
#pragma unroll
    for (int n = 0; n < 5; ++n) gw[n] = *(const u32x4*)(p.gw + (n * 16 + i16) * 8);
    sched0();
    waitvm<9>(); bar();
    conv_compute<4, 16, false>(c, L_CONV, dA, W, H, tag, 0, keep, rq, (row0 * D + i16 * 8) * 2 TSPASS);
    {
      f32x4 oth[4];
#pragma unroll
      for (int n = 0; n < 4; ++n) oth[n] = *(LAS f32x4*)(xr + n * 1024);
#pragma unroll
      for (int n = 0; n < 4; ++n) {
        const f32x4 y = keep[n] + oth[n];
        __builtin_amdgcn_raw_buffer_store_b64(u32x2{pk2(y[0], y[1]), pk2(y[2], y[3])}, c.rws, lane * 8, OFF_Z + (cA + (w >> 1)) * 4096 + ((w & 1) * 4 + n) * 512, AUX_SC1);
      }
    }
    sched0();
#pragma unroll
    for (int r = 0; r < 2; ++r) {
      float a = 0.f, a2 = 0.f;
#pragma unroll
      for (int n = 0; n < 5; ++n) { const u32x4 x = H[r * 5 + n]; a = dot2(x.x, gw[n].x, a); a2 = dot2(x.y, gw[n].y, a2); a = dot2(x.z, gw[n].z, a); a2 = dot2(x.w, gw[n].w, a2); }
      a += a2;
      a += __builtin_bit_cast(float, __builtin_amdgcn_update_dpp(0, __builtin_bit_cast(int, a), 0x128, 0xf, 0xf, false));
      a += __builtin_bit_cast(float, __builtin_amdgcn_update_dpp(0, __builtin_bit_cast(int, a), 0x124, 0xf, 0xf, false));
      a += __builtin_bit_cast(float, __builtin_amdgcn_update_dpp(0, __builtin_bit_cast(int, a), 0x122, 0xf, 0xf, false));
      a += __builtin_bit_cast(float, __builtin_amdgcn_update_dpp(0, __builtin_bit_cast(int, a), 0x121, 0xf, 0xf, false));
      const float gg = __builtin_amdgcn_rcpf(1.f + __builtin_amdgcn_exp2f(-1.4426950408889634f * a));
      __builtin_amdgcn_raw_buffer_store_b32(__builtin_bit_cast(unsigned, gg), c.rws, i16 == 0 ? (unsigned)(row0 + r * 4) * 4 : OOB, OFF_G, AUX_SC1);
    }
    sched0();
    bar();
  } else {
    dbits = __builtin_amdgcn_raw_buffer_load_b16(rsd, 0, (c0 + (w >> 1)) * 2, 0);
    sched0();
    phase_g1(p, c, rt, ct, c6 + r6, xcd + (xcd >> 2), b, W, tag TSPASS);
    asm volatile("" :: "v"(pf));
  }
  TS(13);
  const int g = lane >> 4, c16 = lane & 15, q4 = (lane >> 2) & 3, p4 = lane & 3;
  const int s = R * 64 + 32 * ((c16 >> 1) & 1) + 8 * w + 2 * (c16 >> 2) + (c16 & 1), d0 = cb * 80;
  const unsigned qo = opq((s * D + d0 + 8 * g) * 2), qo4 = opq((s * D + d0 + 64 + 4 * g) * 2), go = opq(s * 4);
  const unsigned pr = opq(w * 15360 + lane * 16), zv = opq((lane >> 3) * 4096 + (lane & 7) * 16);
  const unsigned za8 = opq(L_ZI + (8 * g + q4) * 128 + w * 32 + p4 * 8), za4 = opq(L_ZI + (64 + 4 * g + q4) * 128 + w * 32 + p4 * 8);
  const unsigned xro = opq(xr), l8 = opq(lane * 8);
  const rsrc_t rb2 = mkrsrc(p.b2, D * 2), ro = mkrsrc(p.out, S * D * 2);
  sched0();
  conv_compute<20, 27, true>(c, lbase, dbits, W, H, tag, pub, keep, rq, 0 TSPASS);
  TS(16);
  f32x4 ac2[2][5], part[3][5], oth[4];
  u32x4 q0, q1, e0, e1; u32x2 q2, e2, poll, Z[5]; float gg;
#pragma unroll
  for (int pp = 0; pp < 4; ++pp) {
    f32x4 (&acc)[5] = ac2[pp & 1];
#pragma unroll
    for (int ii = 0; ii < 12; ++ii) {
      const int n = pp * 12 + ii;
#pragma unroll
      for (int T = 0; T < 5; ++T) {
        acc[T] = mfma(W[ii * 5 + T], H[n], ii == 0 ? f32x4{0.f, 0.f, 0.f, 0.f} : acc[T]);
        sched0();
        const int hn = 6 + n + (n > 7 ? (n < 12 ? n : 12) - 7 : 0);
        if (T == 1 && hn < 48) { H[hn] = load_h(c, hn); sched0(); }
        if (T == 3 && n < 7) { W[53 + n] = load_w(c, 53 + n); sched0(); }
        if (T == 3 && n >= 7 && n < 12) { H[hn + 1] = load_h(c, hn + 1); sched0(); }
        if (n == 0 && T != 1) { const int x = T - (T > 1); oth[x] = *(LAS f32x4*)(xro + x * 1024); sched0(); }
        if (n >= 2 && n < 6) {
          if (T == 0) { keep[n - 2][0] += oth[n - 2][0]; keep[n - 2][1] += oth[n - 2][1]; sched0(); }
          if (T == 2) { keep[n - 2][2] += oth[n - 2][2]; keep[n - 2][3] += oth[n - 2][3]; sched0(); }
          if (T == 3) { poll = u32x2{pk2(keep[n - 2][0], keep[n - 2][1]), pk2(keep[n - 2][2], keep[n - 2][3])}; sched0(); }
          if (T == 4) { __builtin_amdgcn_raw_buffer_store_b64(poll, c.rws, l8, OFF_Z + (c0 + (w >> 1)) * 4096 + ((w & 1) * 4 + n - 2) * 512, AUX_SC1); sched0(); }
        }
        if (T == 3 && pp > 0 && ii < 5) { *(LAS f32x4*)((((w + pp) & 3) * 3 + pp - 1) * 5120 + ii * 1024 + c.l16) = ac2[(pp - 1) & 1][ii]; sched0(); }
        if (T == 3 && pp == 2 && ii >= 5 && ii < 11) {
          if (ii == 5) q0 = __builtin_amdgcn_raw_buffer_load_b128(rq, qo, 0, 0);
          if (ii == 6) q1 = __builtin_amdgcn_raw_buffer_load_b128(rq, qo + 64, 0, 0);
          if (ii == 7) { q2 = __builtin_amdgcn_raw_buffer_load_b64(rq, qo4, 0, 0); poll = flag_load(c.rws, c.p2); }
          if (ii == 8) e0 = __builtin_amdgcn_raw_buffer_load_b128(rb2, g * 16, d0 * 2, 0);
          if (ii == 9) e1 = __builtin_amdgcn_raw_buffer_load_b128(rb2, g * 16 + 64, d0 * 2, 0);
          if (ii == 10) e2 = __builtin_amdgcn_raw_buffer_load_b64(rb2, g * 8 + 128, d0 * 2, 0);
          sched0();
        }
        if (pp == 3 && ii == 0 && T < 3) {
          dma16<2, 0>(c.sws, w * 3 + T < 10 ? zv : OOB, OFF_Z + (d0 + (w * 3 + T) * 8) * 4096 + R * 128, L_ZI + (w * 3 + T) * 1024); sched0();
        }
        if (pp == 3 && ii == 0 && T == 4) { gg = __builtin_bit_cast(float, __builtin_amdgcn_raw_buffer_load_b32(c.rws, go, OFF_G, AUX_SC1)); sched0(); }
        if (pp == 3 && ii >= 8 && ii < 11) { part[ii - 8][T] = *(LAS f32x4*)(pr + (ii - 8) * 5120 + T * 1024); sched0(); }
        if (pp == 3 && ii == 11) { Z[T] = T < 4 ? ldstr(za8 + (32 * (T >> 1) + 4 * (T & 1)) * 128) : ldstr(za4); sched0(); }
      }
      if (n == 13) { waitvm<14>(); bar(); flag_store(c.rws, c.vrep, F20 + pairB, tag); sched0(); }
      if (n == 35) { TS(18); flag_wait(c.rws, c.p2, poll, tag); bar(); TS(19); }
      if (n == 43) { waitlgkm0(); waitvm<1>(); bar(); }
    }
  }
  TS(20);
  f32x4 (&acc)[5] = ac2[1];
#pragma unroll
  for (int T = 0; T < 5; ++T) acc[T] += (part[0][T] + part[1][T]) + part[2][T];
  const float og = 1.f - gg;
  const unsigned qq[10] = {q0.x, q0.y, q0.z, q0.w, q1.x, q1.y, q1.z, q1.w, q2.x, q2.y};
  const unsigned ee[10] = {e0.x, e0.y, e0.z, e0.w, e1.x, e1.y, e1.z, e1.w, e2.x, e2.y};
  unsigned o[10];
#pragma unroll
  for (int j = 0; j < 10; ++j) {
    const unsigned zz = (j & 1) ? Z[j >> 1].y : Z[j >> 1].x;
    const float b0 = acc[j >> 1][(j & 1) * 2] + blo(ee[j]), b1 = acc[j >> 1][(j & 1) * 2 + 1] + bhi(ee[j]);
    o[j] = pk2(gg * (blo(qq[j]) * blo(zz)) + og * b0, gg * (bhi(qq[j]) * bhi(zz)) + og * b1);
  }
  __builtin_amdgcn_raw_buffer_store_b128(u32x4{o[0], o[1], o[2], o[3]}, ro, qo, 0, 0);
  __builtin_amdgcn_raw_buffer_store_b128(u32x4{o[4], o[5], o[6], o[7]}, ro, qo + 64, 0, 0);
  __builtin_amdgcn_raw_buffer_store_b64(u32x2{o[8], o[9]}, ro, qo4, 0, 0);
  TS(21);
  if (b == 255 && w == 0) {
#pragma unroll 1
    for (int n = 0; n < 5; ++n) { const unsigned vo = prep_ + (F20 + n * 64 + lane) * FL_STR; flag_wait(c.rws, vo, flag_load(c.rws, vo), tag); }
    __builtin_amdgcn_raw_buffer_store_b64(tag, c.rws, lane == 0 ? 0u : OOB, OFF_E, AUX_SC1);
  }
}

extern "C" void kernel_launch(void* const* d_in, const int* in_sizes, int n_in, void* d_out, int out_size, void* d_ws, size_t ws_size, hipStream_t stream) {
  Params p;
  p.q = (const u16*)d_in[0]; p.k = (const u16*)d_in[1]; p.v = (const u16*)d_in[2]; p.sk = (const u16*)d_in[3];
  p.fk = (const u16*)d_in[4]; p.ssd = (const u16*)d_in[5]; p.w1 = (const u16*)d_in[7]; p.b1 = (const u16*)d_in[8];
  p.w2 = (const u16*)d_in[9]; p.b2 = (const u16*)d_in[10]; p.gw = (const u16*)d_in[11];
  p.out = (u16*)d_out; p.ws = (char*)d_ws;
  (void)hipFuncSetAttribute((const void*)k_fused, hipFuncAttributeMaxDynamicSharedMemorySize, LDS_BYTES);
  k_fused<<<256, 256, LDS_BYTES, stream>>>(p);
}
```

```cpp
#include <hip/hip_runtime.h>
#include <stdint.h>

typedef unsigned short u16;
typedef short s16x8 __attribute__((ext_vector_type(8)));
typedef short s16x4 __attribute__((ext_vector_type(4)));
typedef float f32x4 __attribute__((ext_vector_type(4)));
typedef float f32x2 __attribute__((ext_vector_type(2)));
typedef __bf16 bf16x2 __attribute__((ext_vector_type(2)));
typedef unsigned u32x4 __attribute__((ext_vector_type(4)));
typedef unsigned u32x2 __attribute__((ext_vector_type(2)));
typedef int i32x4 __attribute__((ext_vector_type(4)));
#define LAS __attribute__((address_space(3)))
#define DEV __device__ __forceinline__
typedef __amdgpu_buffer_rsrc_t rsrc_t;

constexpr int S = 2048, D = 640, F = 1536;
constexpr unsigned RW_PITCH = 4096 + 128;
constexpr unsigned OFF_E = 0, OFF_FL = 4096, FL_REP = 33024, FL_STR = 32;
constexpr unsigned FK0 = 0, FW0 = 64, F10 = 320, F20 = 512;
constexpr unsigned OFF_RW = 1u << 20, OFF_W2T = 4u << 20, OFF_H = 6u << 20, OFF_Z = 13u << 20, OFF_G = 16u << 20, WS_USED = 17u << 20;
constexpr int LDS_BYTES = 160 * 1024;
constexpr unsigned L_CONV = 98304, CONV_CH = 17600, L_XCH = 133504, L_TO = 149888, L_ZI = 65536;
constexpr int AUX_SC1 = 16;
constexpr unsigned OOB = 0x80000000u, SPIN_CAP = 1u << 16;
constexpr unsigned CODE_BYTES = 30 * 1024;

struct Params {
  const u16 *q, *k, *v, *sk, *fk, *ssd, *w1, *b1, *w2, *b2, *gw;
  u16* out;
  char* ws;
};

DEV f32x4 mfma(u32x4 a, u32x4 b, f32x4 c) { return __builtin_amdgcn_mfma_f32_16x16x32_bf16(__builtin_bit_cast(s16x8, a), __builtin_bit_cast(s16x8, b), c, 0, 0, 0); }
DEV i32x4 mksrd(const void* p, unsigned bytes) {
  unsigned long long a = (unsigned long long)p;
  i32x4 r; r.x = (int)(unsigned)a; r.y = (int)(unsigned)(a >> 32); r.z = (int)bytes; r.w = 0x00020000; return r;
}
DEV rsrc_t mkrsrc(const void* p, unsigned bytes) { return __builtin_amdgcn_make_buffer_rsrc((void*)p, 0, (int)bytes, 0x00020000); }
template <int POL, int IMM> DEV void dma16(i32x4 srd, unsigned voff, unsigned soff, unsigned lds) {
  if (POL == 0) asm volatile("s_mov_b32 m0, %2\n\ts_nop 0\n\tbuffer_load_dwordx4 %0, %1, %3 offen offset:%4 lds" :: "v"(voff), "s"(srd), "s"(lds), "s"(soff), "i"(IMM) : "memory");
  if (POL == 1) asm volatile("s_mov_b32 m0, %2\n\ts_nop 0\n\tbuffer_load_dwordx4 %0, %1, %3 offen offset:%4 nt lds" :: "v"(voff), "s"(srd), "s"(lds), "s"(soff), "i"(IMM) : "memory");
  if (POL == 2) asm volatile("s_mov_b32 m0, %2\n\ts_nop 0\n\tbuffer_load_dwordx4 %0, %1, %3 offen offset:%4 sc1 lds" :: "v"(voff), "s"(srd), "s"(lds), "s"(soff), "i"(IMM) : "memory");
}
template <int POL> DEV void dma16n(i32x4 srd, unsigned voff, unsigned soff, unsigned lds, int n) {
  if (n == 0) dma16<POL, 0>(srd, voff, soff, lds);
  if (n == 1) dma16<POL, 1024>(srd, voff, soff, lds);
  if (n == 2) dma16<POL, 2048>(srd, voff, soff, lds);
  if (n == 3) dma16<POL, 3072>(srd, voff, soff, lds);
}
DEV void setm0(unsigned lds) { asm volatile("s_mov_b32 m0, %0" :: "s"(lds) : "memory"); }
template <int IMM> DEV void dma16m(i32x4 srd, unsigned voff, unsigned soff) {
  asm volatile("buffer_load_dwordx4 %0, %1, %2 offen offset:%3 lds" :: "v"(voff), "s"(srd), "s"(soff), "i"(IMM) : "memory");
}
template <int N> DEV void waitvm() { asm volatile("s_waitcnt vmcnt(%0)" :: "i"(N) : "memory"); }
DEV void waitlgkm0() { asm volatile("s_waitcnt lgkmcnt(0)" ::: "memory"); }
DEV void bar() { asm volatile("s_barrier" ::: "memory"); }
DEV void sched0() { __builtin_amdgcn_sched_barrier(0); }
DEV unsigned opq(unsigned x) { asm volatile("" : "+v"(x)); return x; }
DEV unsigned opqs(unsigned x) { asm volatile("" : "+s"(x)); return x; }
DEV u32x4 lds128(unsigned a) { return *(LAS u32x4*)a; }
DEV u32x2 lds64(unsigned a) { return *(LAS u32x2*)a; }
DEV u32x2 ldstr(unsigned a) { return __builtin_bit_cast(u32x2, __builtin_amdgcn_ds_read_tr16_b64_v4i16((LAS s16x4*)a)); }
DEV unsigned pk2(float lo, float hi) { f32x2 z = {lo, hi}; return __builtin_bit_cast(unsigned, __builtin_convertvector(z, bf16x2)); }
DEV float blo(unsigned w) { return __builtin_bit_cast(float, w << 16); }
DEV float bhi(unsigned w) { return __builtin_bit_cast(float, w & 0xffff0000u); }
DEV float dot2(unsigned a, unsigned b, float c) { return __builtin_amdgcn_fdot2_f32_bf16(__builtin_bit_cast(bf16x2, a), __builtin_bit_cast(bf16x2, b), c, false); }
DEV void st128(u32x4 v, rsrc_t r, unsigned voff, unsigned soff) { __builtin_amdgcn_raw_buffer_store_b128(v, r, voff, soff, AUX_SC1); sched0(); asm volatile("s_nop 1"); sched0(); }

#define TS(idx) do {} while (0)
#define TSDECL
#define TSARG
#define TSPASS
#define TSFLUSH(lo, hi)

DEV u32x2 flag_load(rsrc_t rws, unsigned voff) { return __builtin_amdgcn_raw_buffer_load_b64(rws, voff, OFF_FL, AUX_SC1); }
DEV void flag_wait(rsrc_t rws, unsigned voff, u32x2 first, u32x2 tag) {
  bool bad = (int)voff >= 0 && (first.x != tag.x || first.y != tag.y);
  if (__builtin_expect(__builtin_amdgcn_ballot_w64(bad) != 0ull, 0)) {
    unsigned spins = 0;
#pragma unroll 1
    do {
      __builtin_amdgcn_s_sleep(2);
      asm volatile("" ::: "memory");
      const u32x2 v = flag_load(rws, bad ? voff : OOB);
      bad = bad && (v.x != tag.x || v.y != tag.y);
    } while (__builtin_amdgcn_ballot_w64(bad) != 0ull && ++spins < SPIN_CAP);
  }
  asm volatile("" ::: "memory");
}
DEV void flag_store(rsrc_t rws, unsigned vrep, unsigned idx, u32x2 tag) { __builtin_amdgcn_raw_buffer_store_b64(tag, rws, vrep, OFF_FL + idx * FL_STR, AUX_SC1); }

struct Ctx {
  rsrc_t rws; i32x4 sws;
  int w, lane;
  unsigned l16;
  unsigned vrep;
  unsigned pk, pw, p1, p2;
  unsigned wbase, hbase;
};
DEV u32x4 load_w(const Ctx& c, int n) { return __builtin_amdgcn_raw_buffer_load_b128(c.rws, c.l16 + (n & 3) * 1024, c.wbase + (n >> 2) * 4096, AUX_SC1); }
DEV u32x4 load_h(const Ctx& c, int n) {
  return __builtin_amdgcn_raw_buffer_load_b128(c.rws, c.l16 + (n & 3) * 1024, c.hbase + (((c.w + 1 + n / 12) & 3) * 48 + ((n % 12) & ~3)) * 1024, AUX_SC1);
}

DEV void conv_pads(unsigned lbase, int w, int lane) {
  const unsigned cbase = lbase + (w >> 1) * CONV_CH, half = w & 1;
  const unsigned rb = cbase + half * 4576, fb = cbase + 9152 + half * 4224;
  const int l = lane & 31;
  *(LAS u32x4*)(l < 28 ? rb + 4096 + l * 16 : fb + (l - 28) * 16) = u32x4{0u, 0u, 0u, 0u};
}
DEV void conv_load_f(const Params& p, const Ctx& c, int c0, unsigned lbase, int n) {
  const int half = c.w & 1;
  dma16n<1>(mksrd(p.fk, D * S * 2), c.l16, (c0 + (c.w >> 1)) * 4096, lbase + (c.w >> 1) * CONV_CH + 9152 + 60 + half * 4228, n);
}
DEV void conv_load_r(const Ctx& c, int c0, unsigned lbase, int n) {
  const int half = c.w & 1;
  dma16n<2>(c.sws, c.l16, OFF_RW + (c0 + (c.w >> 1)) * RW_PITCH + 2 - 2 * half, lbase + (c.w >> 1) * CONV_CH + half * 4576, n);
}

DEV void phase_p0(const Params& p, const Ctx& c, int hz, int cA, u32x2& tag TSARG) {
  const int w = c.w, lane = c.lane;
  const i32x4 sk_ = mksrd(p.k, S * D * 2), sv_ = mksrd(p.v, S * D * 2), sw2 = mksrd(p.w2, F * D * 2);
  const int rt = hz >> 1, hf = hz & 1, s0 = rt * 64;
  const int g = lane >> 4, q4 = (lane >> 2) & 3, p4 = lane & 3, i16 = lane & 15;
  constexpr unsigned SLAB = 17408, L_W2 = 87040;
  const unsigned vrow = (lane >> 3) * 1280 + (((lane & 7) * 16) ^ ((g & 1) * 32)) + hf * 640;
  const unsigned vrA = vrow ^ ((w & 1) * 64), vrB = (vrA ^ 64) + 8 * 1280 - 1024, vrC = vrA + 8 * 1280 - 1024;
  auto slab = [&](int j, int n) {
    const unsigned so = (s0 + w * 16) * 1280 + j * 128;
    if (n == 0) dma16<1, 0>(sk_, vrA, so, j * SLAB + 1024 + w * 2048);
    if (n == 1) dma16<1, 1024>(sk_, vrB, so, j * SLAB + 1024 + w * 2048);
    if (n == 2) dma16<1, 0>(sv_, vrA, so, j * SLAB + 9216 + w * 2048);
    if (n == 3) dma16<1, 1024>(sv_, vrC, so, j * SLAB + 9216 + w * 2048);
  };
  const unsigned w2b = hz < 48 ? hz * 40960 : OOB;
#pragma unroll
  for (int n = 0; n < 10; ++n) dma16<1, 0>(sw2, (c.l16 ^ ((((4 * n + g) * 13) >> 6) * 32)) + w2b, (w * 10 + n) * 1024, L_W2 + (w * 10 + n) * 1024);
  u32x2 tap[5];
#pragma unroll
  for (int j = 0; j < 5; ++j) asm volatile("global_load_dwordx2 %0, %1, off offset:%2" : "=v"(tap[j]) : "v"(p.sk + (hf * 320 + w * 16 + i16) * 4), "i"(j * 512) : "memory");
  asm volatile("buffer_load_dwordx2 %0, off, %1, 0 sc1" : "=v"(tag) : "s"(c.sws) : "memory");
  sched0();
  unsigned ka[2][8], va[2];
#pragma unroll
  for (int x = 0; x < 8; ++x) {
    const unsigned row = 8 + 16 * (i16 >> 2) + 4 * (x >> 1) + (i16 & 3) - 2 * (x & 1) - (g >> 1);
    ka[0][x] = opq(row * 128 + ((w ^ (((row >> 1) & 1) | (((row >> 4) & 1) << 1))) * 32) + (g & 1) * 16);
    ka[1][x] = opq(ka[0][x] + 2 * SLAB);
  }
  unsigned msk[4];
#pragma unroll
  for (int d = 0; d < 4; ++d) msk[d] = opq(((i16 >> 3) == (g & 1) && ((i16 & 7) >> 1) == d) ? (0xffffu << (16 * (i16 & 1))) : 0u);
  const unsigned sel = opq((g >> 1) ? 0x03020302u : 0x01000100u);
  va[0] = opq(9216 + (16 * g + q4) * 128 + (w ^ ((q4 >> 1) | ((g & 1) << 1))) * 32 + p4 * 8); va[1] = opq(va[0] + 2 * SLAB);
  const unsigned ta = opq(L_TO + w * 2304 + i16 * 144 + (96 - 32 * g));
  const unsigned ra = opq(L_TO + w * 2304 + (lane >> 3) * 144 + (lane & 7) * 16);
  const unsigned vo = opq((lane >> 3) * RW_PITCH + (lane & 7) * 16);
  const unsigned so0 = OFF_RW + (hf * 320 + w * 16) * RW_PITCH + (1984 - s0) * 2;
  unsigned wa[10];
#pragma unroll
  for (int n = 0; n < 10; ++n) {
    const int T = n % 5; const unsigned row = 4 * g + q4;
    const unsigned col = (w * 2 + n / 5) * 80 + (T < 4 ? 32 * (T >> 1) + 8 * p4 + 4 * (T & 1) : 64 + 4 * p4);
    wa[n] = opq(L_W2 + row * 1280 + ((col * 2) ^ ((row & 7) * 32)));
  }
  const unsigned wo = opq(hz < 48 ? c.l16 : OOB);
  sched0();
  dma16<1, 0>(sk_, vrow + (unsigned)((s0 - 8) * 1280) + w * 128, 0, w * SLAB);
  dma16<1, 0>(sk_, vrow + (unsigned)((s0 - 8) * 1280) + 4 * 128, 0, 4 * SLAB);
#pragma unroll
  for (int n = 0; n < 8; ++n) slab(n >> 2, n & 3);
  u32x4 K[2][8]; u32x2 V[2][4]; u32x4 O[10], B0[5], B1[5];
  auto rd = [&](int j, int x) {
    const int hi = j >= 2; const unsigned jb = (j - 2 * hi) * SLAB;
    if (x < 8) K[j & 1][x] = lds128(ka[hi][x] + jb); else V[j & 1][x - 8] = ldstr(va[hi] + jb + (x - 8) * 512);
  };
  auto put = [&](int n) { st128(O[n], c.rws, vo, so0 + ((n >> 1) * 64 + (n & 1) * 8) * RW_PITCH); };
  TS(1);
  waitvm<10>(); bar();
  TS(2);
  u32x2 lo[10], hi[10];
#pragma unroll
  for (int n = 0; n < 10; ++n) { lo[n] = ldstr(wa[n]); hi[n] = ldstr(wa[n] + 16 * 1280); }
  asm volatile("v_add_u32 %0, 1, %0" : "+v"(tag.x), "+v"(tag.y));
  sched0();
#pragma unroll
  for (int j = 0; j < 5; ++j) {
    asm volatile("" : "+v"(tap[j]));
    const unsigned r01 = __builtin_amdgcn_perm(tap[j].x, tap[j].x, sel), r23 = __builtin_amdgcn_perm(tap[j].y, tap[j].y, sel);
    B0[j] = u32x4{r01 & msk[0], r01 & msk[1], r01 & msk[2], r01 & msk[3]}; B1[j] = u32x4{r23 & msk[0], r23 & msk[1], r23 & msk[2], r23 & msk[3]};
    asm volatile("" : "+v"(B0[j]), "+v"(B1[j]));
    if (j < 4) { slab(2, j); slab(3, j); slab(4, j); }
    sched0();
  }
  auto putw = [&](int n) { st128(u32x4{lo[n].x, lo[n].y, hi[n].x, hi[n].y}, c.rws, wo, OFF_W2T + (((w * 2 + n / 5) * 48 + hz) * 5 + n % 5) * 1024); };
  waitvm<16>(); bar();
  TS(3);
#pragma unroll
  for (int x = 0; x < 12; ++x) rd(0, x);
  sched0();
#pragma unroll
  for (int j = 0; j < 5; ++j) {
    auto vm = [&](int k) {
      if (j == 0) putw(k);
      if (j == 1 && k < 4) putw(6 + k);
      if (j == 2 && k < 4) put(k);
      if (j >= 3 && k >= 2 && k < 4) put(2 * j - 4 + k);
      sched0();
    };
    if (j > 0) { O[2 * j - 2] = lds128(ra); O[2 * j - 1] = lds128(ra + 8 * 144); }
    sched0();
    if (j == 0) { waitvm<12>(); bar(); }
    if (j == 1) { waitvm<6>(); bar(); }
    if (j == 3) { waitvm<4>(); bar(); flag_store(c.rws, hz < 48 ? c.vrep : OOB, FW0 + hz, tag); sched0(); }
    f32x4 ks[4]; f32x2 vf[8];
#pragma unroll
    for (int x = 0; x < 8; ++x) {
      const int t = x & 3;
      ks[t] = x < 4 ? mfma(K[j & 1][2 * t], B0[j], f32x4{0.f, 0.f, 0.f, 0.f}) : mfma(K[j & 1][2 * t + 1], B1[j], ks[t]);
      sched0();
      if (j < 4) rd(j + 1, x < 4 ? 2 * x : 2 * (x - 4) + 1);
      const unsigned vv = (x & 1) ? V[j & 1][x >> 1].y : V[j & 1][x >> 1].x;
      vf[x] = f32x2{blo(vv), bhi(vv)};
      asm volatile("" : "+v"(vf[x]));
      sched0();
      if (x == 3 || x == 7) vm(x >> 2);
    }
    asm volatile("" :: "v"(K[j & 1][0]), "v"(K[j & 1][1]), "v"(K[j & 1][2]), "v"(K[j & 1][3]), "v"(K[j & 1][4]), "v"(K[j & 1][5]), "v"(K[j & 1][6]), "v"(K[j & 1][7]));
    if (j < 4) { rd(j + 1, 8); rd(j + 1, 9); rd(j + 1, 10); rd(j + 1, 11); }
    sched0();
    asm volatile("" : "+a"(ks[0]), "+a"(ks[1]), "+a"(ks[2]), "+a"(ks[3]));
    f32x2 pr[8];
#pragma unroll
    for (int x = 0; x < 8; ++x) { pr[x] = f32x2{ks[x >> 1][(x & 1) * 2], ks[x >> 1][(x & 1) * 2 + 1]}; asm volatile("" : "+v"(pr[x])); }
    sched0();
    vm(2);
    unsigned r[8];
#pragma unroll
    for (int x = 0; x < 8; ++x) pr[x] *= vf[x];
    sched0();
    vm(3);
#pragma unroll
    for (int x = 0; x < 8; ++x) asm volatile("v_cvt_pk_bf16_f32 %0, %1, %2" : "=v"(r[7 - x]) : "v"(pr[x].y), "v"(pr[x].x));
    sched0();
    vm(4);
    *(LAS u32x4*)ta = u32x4{r[0], r[1], r[2], r[3]}; *(LAS u32x4*)(ta + 16) = u32x4{r[4], r[5], r[6], r[7]};
    sched0();
    vm(5);
  }
  O[8] = lds128(ra); O[9] = lds128(ra + 8 * 144);
  put(8); put(9);
  st128(u32x4{0u, 0u, 0u, 0u}, c.rws, threadIdx.x < 10 ? threadIdx.x * RW_PITCH + 4096 : OOB, OFF_RW + hz * 10 * RW_PITCH);
}

template <int W0, int NW, bool LAST>
DEV void conv_compute(const Ctx& c, unsigned lbase, unsigned dbits_in, u32x4 (&W)[60], u32x4 (&H)[48], u32x2 tag, unsigned pub, f32x4 (&keep)[4], rsrc_t rq, unsigned gq TSARG) {
  const int w = c.w, lane = c.lane, half = w & 1, g = lane >> 4, n16 = lane & 15;
  const unsigned cbase = lbase + (w >> 1) * CONV_CH;
  const unsigned ua = opq(cbase + (n16 & 1) * 4576 + 448 - 64 * (n16 >> 1) + 16 * g + 256 * half);
  const unsigned fa = opq(cbase + 9152 + (n16 & 1) * 4228 + 4 * n16 + 16 * g + 256 * half);
  const unsigned fa2 = opq(fa + 8);
  const unsigned xw = opq(L_XCH + w * 4096 + c.l16);
  u32x4 U[2][8]; u32x2 Fl[2][8], Fh[2][8];
  f32x4 acc[8];
  u32x2 poll;
#pragma unroll
  for (int j = 0; j < 8; ++j) { U[0][j] = lds128(ua + 512 * (7 - j)); sched0(); }
  const unsigned dbits = half ? 0u : dbits_in;
  const int dsel = 15 - n16 - 4 * g;
  const u32x4 Df = {dsel == 0 ? dbits : 0u, dsel == 1 ? dbits : 0u, dsel == 2 ? dbits : 0u, dsel == 3 ? dbits : 0u};
  sched0();
#pragma unroll
  for (int j = 0; j < 8; ++j) {
    Fl[0][j] = lds64(fa + 512 * j); Fh[0][j] = lds64(fa2 + 512 * j); sched0();
    acc[j] = mfma(U[0][j], Df, f32x4{0.f, 0.f, 0.f, 0.f}); sched0();
  }
  int wn = 0;
#pragma unroll
  for (int r = 0; r < 3; ++r) {
    const int cur = r & 1, nxt = cur ^ 1;
    int m = 0;
#pragma unroll
    for (int E = 0; E < 8; ++E)
#pragma unroll
      for (int I = E; I < 8; ++I) {
        acc[I] = mfma(U[cur][I - E], u32x4{Fl[cur][E].x, Fl[cur][E].y, Fh[cur][E].x, Fh[cur][E].y}, acc[I]);
        sched0();
        if (m < 32 && (m & 3) != 3) {
          const int x = (m >> 2) * 3 + (m & 3);
          if (x % 3 == 0) U[nxt][x / 3] = lds128(ua + 512 * (7 - x / 3) + 64 * (r + 1));
          if (x % 3 == 1) Fl[nxt][x / 3] = lds64(fa + 512 * (x / 3) + 64 * (r + 1));
          if (x % 3 == 2) Fh[nxt][x / 3] = lds64(fa2 + 512 * (x / 3) + 64 * (r + 1));
          sched0();
        }
        if ((m & 3) == 3 && wn < NW) { W[W0 + wn] = load_w(c, W0 + wn); ++wn; sched0(); }
        if (LAST && r == 2 && m == 21) { poll = flag_load(c.rws, c.p1); sched0(); }
        if (!LAST && r == 2 && m >= 11 && (m & 7) == 3) { conv_load_r(c, pub, 0, (m - 11) >> 3); sched0(); }
        ++m;
      }
    if (LAST && r == 0) { waitvm<9>(); bar(); flag_store(c.rws, c.vrep, pub, tag); sched0(); }
  }
  if (LAST) { TS(14); flag_wait(c.rws, c.p1, poll, tag); bar(); TS(15); }
  auto extra = [&](int m) {
    if (LAST) { if (m % 3 == 2) { const int x = m / 3; if (x & 1) W[W0 + NW + (x >> 1)] = load_w(c, W0 + NW + (x >> 1)); else H[x >> 1] = load_h(c, x >> 1); sched0(); } }
    else if (m % 3 == 2 && m < 30) { H[m / 3] = __builtin_amdgcn_raw_buffer_load_b128(rq, gq + ((m / 3) % 5) * 256, (m / 15) * 4 * D * 2, 0); sched0(); }
  };
  if (half) {
    int m = 0;
#pragma unroll
    for (int E = 0; E < 8; ++E)
#pragma unroll
      for (int I = E; I < 8; ++I) {
        acc[I] = mfma(U[1][I - E], u32x4{Fl[1][E].x, Fl[1][E].y, Fh[1][E].x, Fh[1][E].y}, acc[I]);
        sched0(); extra(m);
        if (I == 7 && E < 4) { *(LAS f32x4*)(xw + E * 1024) = acc[E]; sched0(); }
        ++m;
      }
#pragma unroll
    for (int i = 0; i < 4; ++i) { keep[i] = acc[4 + i]; asm volatile("" : "+v"(keep[i])); }
  } else {
    int m = 0;
#pragma unroll
    for (int P = 0; P < 2; ++P)
#pragma unroll
      for (int E = 0; E < 8; ++E)
#pragma unroll
        for (int I = 4 - 4 * P; I < 8 - 4 * P; ++I)
          if (I >= E) {
            acc[I] = mfma(U[1][I - E], u32x4{Fl[1][E].x, Fl[1][E].y, Fh[1][E].x, Fh[1][E].y}, acc[I]);
            sched0(); extra(m);
            if (m == 25 || m == 27 || m == 28 || m == 30) { const int i = m == 25 ? 0 : m == 27 ? 1 : m == 28 ? 2 : 3; *(LAS f32x4*)(xw + i * 1024) = acc[4 + i]; sched0(); }
            ++m;
          }
#pragma unroll
    for (int i = 0; i < 4; ++i) { keep[i] = acc[i]; asm volatile("" : "+v"(keep[i])); }
  }
  waitlgkm0(); bar();
}

DEV void phase_g1(const Params& p, const Ctx& c, int rt, int ct, int off, int xs, int gi, u32x4 (&W)[60], u32x2& tag TSARG) {
  const int w = c.w, lane = c.lane, c0 = gi * 2;
  const i32x4 sx = mksrd(p.q, S * D * 2), sw = mksrd(p.w1, D * F * 2);
  const int g = lane >> 4, q4 = (lane >> 2) & 3, p4 = lane & 3, n16 = lane & 15, wf = w & 1, wsn = w >> 1;
  unsigned vx[4], vw[4];
#pragma unroll
  for (int j = 0; j < 4; ++j) {
    vx[j] = (rt * 128 + w * 32 + (lane >> 3)) * 1280 + ((lane & 7) ^ ((g << 1) | (w & 1))) * 16 + j * (8 * 1280 - 1024);
    vw[j] = (((w * 16 + g) * 3072 + ct * 256 + (n16 ^ (g << 1)) * 16) ^ ((j >> 1) * 128)) + j * (4 * 3072 - 1024);
  }
  auto kstage = [&](int st) {
    int ks = (st & ~3) + ((st + off) & (st < 8 ? 3 : 1)) + xs; if (ks >= 10) ks -= 10; return ks;
  };
  unsigned sox = 0, sow = 0;
  auto prep = [&](int st, int op) {
    if (op == 0) sox = opqs(kstage(st) * 128); else sow = opqs(kstage(st) * 64 * 3072);
    setm0((st % 3) * 32768 + w * 4096 + op * 16384);
  };
  auto issue = [&](int j) {
    if (j == 0) dma16m<0>(sx, vx[0], sox);
    if (j == 1) dma16m<1024>(sx, vx[1], sox);
    if (j == 2) dma16m<2048>(sx, vx[2], sox);
    if (j == 3) dma16m<3072>(sx, vx[3], sox);
    if (j == 4) dma16m<0>(sw, vw[0], sow);
    if (j == 5) dma16m<1024>(sw, vw[1], sow);
    if (j == 6) dma16m<2048>(sw, vw[2], sow);
    if (j == 7) dma16m<3072>(sw, vw[3], sow);
  };
  auto stage_all = [&](int st) {
    prep(st, 0); asm volatile("s_nop 0"); issue(0); issue(1); issue(2); issue(3);
    prep(st, 1); asm volatile("s_nop 0"); issue(4); issue(5); issue(6); issue(7);
  };
  unsigned touch;
  {
    const int u = (gi >> 3) * 4 + w, xcd = gi & 7;
    const unsigned L = (u & 31) * 64 + lane;
    const unsigned hzk = (L >> 8) * 8 + xcd, okv = ((hzk >> 1) * 64 + ((L >> 2) & 63)) * 1280 + (hzk & 1) * 640 + (1 + (L & 3)) * 128;
    const unsigned h2 = ((u & 31) * 52) >> 8, ow2 = h2 < 6 ? (h2 * 8 + xcd) * 40960 + (L - h2 * 320) * 128 : OOB;
    const i32x4 st = mksrd(u < 32 ? p.k : u < 64 ? p.v : p.w2, u < 64 ? S * D * 2 : F * D * 2);
    asm volatile("buffer_load_dword %0, %1, %2, 0 offen nt" : "=v"(touch) : "v"(u < 64 ? okv : ow2), "s"(st) : "memory");
  }
  stage_all(0);
  u32x2 bias[4];
#pragma unroll
  for (int a = 0; a < 4; ++a) asm volatile("global_load_dwordx2 %0, %1, off offset:%2" : "=v"(bias[a]) : "v"(p.b1 + ct * 128 + wf * 64 + g * 4), "i"(a * 32) : "memory");
  sched0();
  conv_pads(L_CONV, w, lane);
  unsigned wa[2][4], xa[2][2];
#pragma unroll
  for (int a = 0; a < 4; ++a) {
    wa[0][a] = opq(16384 + (8 * g + q4) * 256 + p4 * 8 + ((((wf ^ (g & 1)) << 2) | (a ^ q4)) * 32));
    wa[1][a] = opq(wa[0][a] + 65536);
  }
  xa[0][0] = opq((wsn * 64 + 32 * ((n16 >> 1) & 1) + 2 * (n16 >> 2) + (n16 & 1)) * 128 + ((g ^ (n16 >> 1)) * 16)); xa[0][1] = opq(xa[0][0] ^ 64);
  xa[1][0] = opq(xa[0][0] + 65536); xa[1][1] = opq(xa[0][1] + 65536);
  sched0();
  stage_all(1);
  asm volatile("buffer_load_dwordx2 %0, off, %1, 0 sc1" : "=v"(tag) : "s"(c.sws) : "memory");
  f32x4 acc[4][4];
  u32x2 Al[2][4], Ah[2][4]; u32x4 B[2][4];
  auto rd = [&](int buf, int st, int kk, int x) {
    const int sl = st % 3, hi = sl == 2;
    const unsigned base = (sl & 1) * 32768;
    constexpr int kind[12] = {1, 2, 0, 0, 1, 0, 2, 0, 1, 2, 1, 2}, idx[12] = {0, 0, 0, 1, 1, 2, 1, 3, 2, 2, 3, 3};
    if (kind[x] == 0) B[buf][idx[x]] = lds128(xa[hi][kk] + base + idx[x] * 1024);
    if (kind[x] == 1) Al[buf][idx[x]] = ldstr(wa[hi][idx[x]] + base + kk * 8192);
    if (kind[x] == 2) Ah[buf][idx[x]] = ldstr(wa[hi][idx[x]] + base + kk * 8192 + 1024);
  };
  waitvm<9>(); bar();
  asm volatile("" :: "v"(touch));
  TS(1);
#pragma unroll
  for (int x = 0; x < 12; ++x) rd(0, 0, 0, x);
#pragma unroll
  for (int a = 0; a < 4; ++a) {
    asm volatile("" : "+v"(bias[a]));
    const f32x4 bv = {blo(bias[a].x), bhi(bias[a].x), blo(bias[a].y), bhi(bias[a].y)};
#pragma unroll
    for (int b = 0; b < 4; ++b) acc[a][b] = bv;
  }
  sched0();
  u32x2 pollk, pollw;
  int wn = 0;
#pragma unroll
  for (int st = 0; st < 10; ++st) {
#pragma unroll
    for (int m = 0; m < 16; ++m) {
      const int a = m >> 2, b = m & 3;
      acc[a][b] = mfma(u32x4{Al[0][a].x, Al[0][a].y, Ah[0][a].x, Ah[0][a].y}, B[0][b], acc[a][b]);
      sched0();
      if (m < 12) { rd(1, st, 1, m); sched0(); }
      if (st == 0) {
        if (m == 0 || m == 8) { prep(2, m >> 3); sched0(); }
        if (m & 1) { issue(m >> 1); sched0(); }
      } else if (st + 2 < 10) {
        if (m == 0) { prep(st + 2, 1); sched0(); }
        if ((m & 3) == 1) { issue(4 + (m >> 2)); sched0(); }
      }
      if (st >= 8 && (m & 3) == 1) { W[wn] = load_w(c, wn); ++wn; sched0(); }
      if (st == 9 && m == 14) { pollk = flag_load(c.rws, c.pk); sched0(); }
    }
    if (st < 9) {
      waitlgkm0();
      waitvm<8>();
      if (st == 0) asm volatile("v_add_u32 %0, 1, %0" : "+v"(tag.x), "+v"(tag.y));
      if (st == 7) { asm volatile("" : "+v"(pollw)); flag_wait(c.rws, c.pw, pollw, tag); }
      bar();
      if (st == 6) asm volatile("buffer_load_dwordx2 %0, %1, %2, %3 offen sc1" : "=v"(pollw) : "v"(c.pw), "s"(c.sws), "s"(OFF_FL) : "memory");
    }
#pragma unroll
    for (int m = 0; m < 16; ++m) {
      const int a = m >> 2, b = m & 3;
      acc[a][b] = mfma(u32x4{Al[1][a].x, Al[1][a].y, Ah[1][a].x, Ah[1][a].y}, B[1][b], acc[a][b]);
      if (st < 9) {
        sched0();
        if (m < 12) { rd(0, st + 1, 0, m); sched0(); }
        if (st + 3 < 10) {
          if (m == 0) { prep(st + 3, 0); sched0(); }
          if ((m & 3) == 1) { issue(m >> 2); sched0(); }
        }
        if (st == 7 && (m & 3) == 1) { conv_load_f(p, c, c0, L_CONV, m >> 2); sched0(); }
        if (st == 8 && (m & 3) == 1) { W[wn] = load_w(c, wn); ++wn; sched0(); }
      } else {
        sched0();
        if ((m & 3) == 1) { W[wn] = load_w(c, wn); ++wn; sched0(); }
      }
    }
  }
#pragma unroll
  for (int a = 0; a < 4; ++a) asm volatile("" : "+a"(acc[a][0]), "+a"(acc[a][1]), "+a"(acc[a][2]), "+a"(acc[a][3]));
  TS(11);
  flag_wait(c.rws, c.pk, pollk, tag); bar();
  TS(12);
  const unsigned ho = OFF_H + ((rt * 8 + wsn * 4) * 48 + ct * 4 + wf * 2) * 1024;
#pragma unroll
  for (int n = 0; n < 8; ++n) {
    const int b = n >> 1, P = n & 1;
    constexpr float c0f = -2.f * 0.7978845608028654f * 1.4426950408889634f, c1f = c0f * 0.044715f;
    f32x2 z[4], t[4];
    asm volatile("" : "+a"(acc[2 * P][b]), "+a"(acc[2 * P + 1][b]));
    z[0] = f32x2{acc[2 * P][b][0], acc[2 * P][b][1]}; z[1] = f32x2{acc[2 * P][b][2], acc[2 * P][b][3]};
    z[2] = f32x2{acc[2 * P + 1][b][0], acc[2 * P + 1][b][1]}; z[3] = f32x2{acc[2 * P + 1][b][2], acc[2 * P + 1][b][3]};
#pragma unroll
    for (int x = 0; x < 4; ++x) asm volatile("" : "+v"(z[x]));
    sched0();
#pragma unroll
    for (int x = 0; x < 4; ++x) t[x] = z[x] * z[x];
    sched0();
#pragma unroll
    for (int x = 0; x < 4; ++x) t[x] = __builtin_elementwise_fma(t[x], f32x2{c1f, c1f}, f32x2{c0f, c0f});
    sched0();
#pragma unroll
    for (int x = 0; x < 4; ++x) t[x] = t[x] * z[x];
    sched0();
#pragma unroll
    for (int x = 0; x < 4; ++x) t[x] = f32x2{__builtin_amdgcn_exp2f(t[x].x), __builtin_amdgcn_exp2f(t[x].y)};
    sched0();
    if (n < 4) conv_load_r(c, c0, L_CONV, n); else W[16 + n - 4] = load_w(c, 16 + n - 4);
    sched0();
#pragma unroll
    for (int x = 0; x < 4; ++x) t[x] = t[x] + f32x2{1.f, 1.f};
    sched0();
#pragma unroll
    for (int x = 0; x < 4; ++x) t[x] = f32x2{__builtin_amdgcn_rcpf(t[x].x), __builtin_amdgcn_rcpf(t[x].y)};
    sched0();
#pragma unroll
    for (int x = 0; x < 4; ++x) t[x] = t[x] * z[x];
    sched0();
    const u32x4 o = {pk2(t[0].x, t[0].y), pk2(t[1].x, t[1].y), pk2(t[2].x, t[2].y), pk2(t[3].x, t[3].y)};
    st128(o, c.rws, c.l16, ho + (b * 48 + P) * 1024);
    sched0();
  }
  waitvm<9>(); bar();
}

__global__ __launch_bounds__(256, 1) void k_fused(Params p) {
  extern __shared__ __attribute__((aligned(16))) char smem[];
  const int b = blockIdx.x, xcd = b & 7, i = b >> 3;
  const int w = __builtin_amdgcn_readfirstlane(threadIdx.x >> 6), lane = threadIdx.x & 63;
  TSDECL; TS(0);
  unsigned pf;
  {
    unsigned long long pc; asm volatile("s_getpc_b64 %0" : "=s"(pc));
    const i32x4 sc = mksrd((const void*)(pc & ~127ull), CODE_BYTES);
    asm volatile("buffer_load_dword %0, %1, %2, 0 offen" : "=v"(pf) : "v"(threadIdx.x < 8 ? (unsigned)(i * 1024 + threadIdx.x * 128) : OOB), "s"(sc) : "memory");
  }
  const bool helper = b < 64;
  const int ri = i >> 3, cb = i & 7, R = xcd * 4 + ri;
  const int pairB = helper ? 193 + 2 * b : b - 64, c0 = pairB * 2;
  const unsigned lbase = helper ? 0u : L_CONV;
  const rsrc_t rsd = mkrsrc(p.ssd, D * 2), rq = mkrsrc(p.q, S * D * 2);
  Ctx c;
  c.rws = mkrsrc(p.ws, WS_USED); c.sws = mksrd(p.ws, WS_USED); c.w = w; c.lane = lane; c.l16 = lane * 16;
  c.wbase = OFF_W2T + (cb * 48 + w * 12) * 5120; c.hbase = OFF_H + (R * 4 * 48 + w * 12) * 1024;
  c.vrep = threadIdx.x < 8 ? threadIdx.x * FL_REP : OOB;
  const unsigned prep_ = xcd * FL_REP;
  c.pk = threadIdx.x < 64 ? prep_ + (FK0 + lane) * FL_STR : OOB;
  c.pw = threadIdx.x < 48 ? prep_ + (FW0 + lane) * FL_STR : OOB;
  c.p1 = threadIdx.x < 12 ? prep_ + (F10 + (R >> 1) * 12 + lane) * FL_STR : OOB;
  c.p2 = threadIdx.x < 42 ? prep_ + (F20 + (lane < 40 ? cb * 40 + lane : 192 + 2 * ((2 * ri + (lane & 1)) * 8 + xcd))) * FL_STR : OOB;
  u32x2 tag;
  u32x4 W[60], H[48];
  f32x4 keep[4];
  unsigned dbits;
  const int r6 = ((i - 8) * 43) >> 8, c6 = i - 8 - r6 * 6;
  const int rt = (xcd >> 1) * 4 + r6, ct = (xcd & 1) * 6 + c6;
  const unsigned pub = helper ? F20 + pairB - 1 : F10 + rt * 12 + ct;
  const unsigned xr = L_XCH + (w ^ 1) * 4096 + lane * 16;
  if (helper) {
    const int hz = b, cA = c0 - 2;
    const unsigned dA = __builtin_amdgcn_raw_buffer_load_b16(rsd, 0, (cA + (w >> 1)) * 2, 0);
    dbits = __builtin_amdgcn_raw_buffer_load_b16(rsd, 0, (c0 + (w >> 1)) * 2, 0);
    sched0();
    phase_p0(p, c, hz, cA, tag TSPASS);
    TS(9);
    conv_pads(L_CONV, w, lane);
    waitlgkm0();
#pragma unroll
    for (int n = 0; n < 4; ++n) conv_load_f(p, c, cA, L_CONV, n);
    waitvm<4>(); bar();
    asm volatile("" :: "v"(pf));
    flag_store(c.rws, c.vrep, FK0 + hz, tag);
    TS(10);
    conv_pads(0, w, lane);
    waitlgkm0();
#pragma unroll
    for (int n = 0; n < 4; ++n) conv_load_f(p, c, c0, 0, n);
    const int g = lane >> 4, i16 = lane & 15, row0 = xcd * 256 + i * 32 + w * 8 + g;
    u32x4 gw[5];
#pragma unroll
    for (int n = 0; n < 5; ++n) gw[n] = *(const u32x4*)(p.gw + (n * 16 + i16) * 8);
    sched0();
    flag_wait(c.rws, c.pk, flag_load(c.rws, c.pk), tag);
    bar();
    TS(11);
#pragma unroll
    for (int n = 0; n < 4; ++n) conv_load_r(c, cA, L_CONV, n);
    sched0();
    flag_wait(c.rws, c.pw, flag_load(c.rws, c.pw), tag);
    waitvm<0>(); bar();
    TS(12);
    conv_compute<0, 20, false>(c, L_CONV, dA, W, H, tag, c0, keep, rq, (row0 * D + i16 * 8) * 2 TSPASS);
    {
      f32x4 oth[4];
#pragma unroll
      for (int n = 0; n < 4; ++n) oth[n] = *(LAS f32x4*)(xr + n * 1024);
#pragma unroll
      for (int n = 0; n < 4; ++n) {
        const f32x4 y = keep[n] + oth[n];
        __builtin_amdgcn_raw_buffer_store_b64(u32x2{pk2(y[0], y[1]), pk2(y[2], y[3])}, c.rws, lane * 8, OFF_Z + (cA + (w >> 1)) * 4096 + ((w & 1) * 4 + n) * 512, AUX_SC1);
      }
    }
    sched0();
#pragma unroll
    for (int r = 0; r < 2; ++r) {
      float a = 0.f, a2 = 0.f;
#pragma unroll
      for (int n = 0; n < 5; ++n) { const u32x4 x = H[r * 5 + n]; a = dot2(x.x, gw[n].x, a); a2 = dot2(x.y, gw[n].y, a2); a = dot2(x.z, gw[n].z, a); a2 = dot2(x.w, gw[n].w, a2); }
      a += a2;
      a += __builtin_bit_cast(float, __builtin_amdgcn_update_dpp(0, __builtin_bit_cast(int, a), 0x128, 0xf, 0xf, false));
      a += __builtin_bit_cast(float, __builtin_amdgcn_update_dpp(0, __builtin_bit_cast(int, a), 0x124, 0xf, 0xf, false));
      a += __builtin_bit_cast(float, __builtin_amdgcn_update_dpp(0, __builtin_bit_cast(int, a), 0x122, 0xf, 0xf, false));
      a += __builtin_bit_cast(float, __builtin_amdgcn_update_dpp(0, __builtin_bit_cast(int, a), 0x121, 0xf, 0xf, false));
      const float gg = __builtin_amdgcn_rcpf(1.f + __builtin_amdgcn_exp2f(-1.4426950408889634f * a));
      __builtin_amdgcn_raw_buffer_store_b32(__builtin_bit_cast(unsigned, gg), c.rws, i16 == 0 ? (unsigned)(row0 + r * 4) * 4 : OOB, OFF_G, AUX_SC1);
    }
    sched0();
    bar();
  } else {
    dbits = __builtin_amdgcn_raw_buffer_load_b16(rsd, 0, (c0 + (w >> 1)) * 2, 0);
    sched0();
    phase_g1(p, c, rt, ct, c6 + r6, xcd + (xcd >> 2), b - 64, W, tag TSPASS);
    asm volatile("" :: "v"(pf));
  }
  TS(13);
  const int g = lane >> 4, c16 = lane & 15, q4 = (lane >> 2) & 3, p4 = lane & 3;
  const int s = R * 64 + 32 * ((c16 >> 1) & 1) + 8 * w + 2 * (c16 >> 2) + (c16 & 1), d0 = cb * 80;
  const unsigned qo = opq((s * D + d0 + 8 * g) * 2), qo4 = opq((s * D + d0 + 64 + 4 * g) * 2), go = opq(s * 4);
  const unsigned pr = opq(w * 15360 + lane * 16), zv = opq((lane >> 3) * 4096 + (lane & 7) * 16);
  const unsigned za8 = opq(L_ZI + (8 * g + q4) * 128 + w * 32 + p4 * 8), za4 = opq(L_ZI + (64 + 4 * g + q4) * 128 + w * 32 + p4 * 8);
  const unsigned xro = opq(xr), l8 = opq(lane * 8);
  const rsrc_t rb2 = mkrsrc(p.b2, D * 2), ro = mkrsrc(p.out, S * D * 2);
  sched0();
  conv_compute<20, 27, true>(c, lbase, dbits, W, H, tag, pub, keep, rq, 0 TSPASS);
  TS(16);
  f32x4 ac2[2][5], part[3][5], oth[4];
  u32x4 q0, q1, e0, e1; u32x2 q2, e2, poll, Z[5]; float gg;
#pragma unroll
  for (int pp = 0; pp < 4; ++pp) {
    f32x4 (&acc)[5] = ac2[pp & 1];
#pragma unroll
    for (int ii = 0; ii < 12; ++ii) {
      const int n = pp * 12 + ii;
#pragma unroll
      for (int T = 0; T < 5; ++T) {
        acc[T] = mfma(W[ii * 5 + T], H[n], ii == 0 ? f32x4{0.f, 0.f, 0.f, 0.f} : acc[T]);
        sched0();
        const int hn = 6 + n + (n > 7 ? (n < 12 ? n : 12) - 7 : 0);
        if (T == 1 && hn < 48) { H[hn] = load_h(c, hn); sched0(); }
        if (T == 3 && n < 7) { W[53 + n] = load_w(c, 53 + n); sched0(); }
        if (T == 3 && n >= 7 && n < 12) { H[hn + 1] = load_h(c, hn + 1); sched0(); }
        if (n == 0 && T != 1) { const int x = T - (T > 1); oth[x] = *(LAS f32x4*)(xro + x * 1024); sched0(); }
        if (n >= 2 && n < 6) {
          if (T == 0) { keep[n - 2][0] += oth[n - 2][0]; keep[n - 2][1] += oth[n - 2][1]; sched0(); }
          if (T == 2) { keep[n - 2][2] += oth[n - 2][2]; keep[n - 2][3] += oth[n - 2][3]; sched0(); }
          if (T == 3) { poll = u32x2{pk2(keep[n - 2][0], keep[n - 2][1]), pk2(keep[n - 2][2], keep[n - 2][3])}; sched0(); }
          if (T == 4) { __builtin_amdgcn_raw_buffer_store_b64(poll, c.rws, l8, OFF_Z + (c0 + (w >> 1)) * 4096 + ((w & 1) * 4 + n - 2) * 512, AUX_SC1); sched0(); }
        }
        if (T == 3 && pp > 0 && ii < 5) { *(LAS f32x4*)((((w + pp) & 3) * 3 + pp - 1) * 5120 + ii * 1024 + c.l16) = ac2[(pp - 1) & 1][ii]; sched0(); }
        if (T == 3 && pp == 2 && ii >= 5 && ii < 11) {
          if (ii == 5) q0 = __builtin_amdgcn_raw_buffer_load_b128(rq, qo, 0, 0);
          if (ii == 6) q1 = __builtin_amdgcn_raw_buffer_load_b128(rq, qo + 64, 0, 0);
          if (ii == 7) { q2 = __builtin_amdgcn_raw_buffer_load_b64(rq, qo4, 0, 0); poll = flag_load(c.rws, c.p2); }
          if (ii == 8) e0 = __builtin_amdgcn_raw_buffer_load_b128(rb2, g * 16, d0 * 2, 0);
          if (ii == 9) e1 = __builtin_amdgcn_raw_buffer_load_b128(rb2, g * 16 + 64, d0 * 2, 0);
          if (ii == 10) e2 = __builtin_amdgcn_raw_buffer_load_b64(rb2, g * 8 + 128, d0 * 2, 0);
          sched0();
        }
        if (pp == 3 && ii == 0 && T < 3) {
          dma16<2, 0>(c.sws, w * 3 + T < 10 ? zv : OOB, OFF_Z + (d0 + (w * 3 + T) * 8) * 4096 + R * 128, L_ZI + (w * 3 + T) * 1024); sched0();
        }
        if (pp == 3 && ii == 0 && T == 4) { gg = __builtin_bit_cast(float, __builtin_amdgcn_raw_buffer_load_b32(c.rws, go, OFF_G, AUX_SC1)); sched0(); }
        if (pp == 3 && ii >= 8 && ii < 11) { part[ii - 8][T] = *(LAS f32x4*)(pr + (ii - 8) * 5120 + T * 1024); sched0(); }
        if (pp == 3 && ii == 11) { Z[T] = T < 4 ? ldstr(za8 + (32 * (T >> 1) + 4 * (T & 1)) * 128) : ldstr(za4); sched0(); }
      }
      if (n == 13) { waitvm<14>(); bar(); flag_store(c.rws, c.vrep, F20 + pairB, tag); sched0(); }
      if (n == 35) { TS(18); flag_wait(c.rws, c.p2, poll, tag); bar(); TS(19); }
      if (n == 43) { waitlgkm0(); waitvm<1>(); bar(); }
    }
  }
  TS(20);
  f32x4 (&acc)[5] = ac2[1];
#pragma unroll
  for (int T = 0; T < 5; ++T) acc[T] += (part[0][T] + part[1][T]) + part[2][T];
  const float og = 1.f - gg;
  const unsigned qq[10] = {q0.x, q0.y, q0.z, q0.w, q1.x, q1.y, q1.z, q1.w, q2.x, q2.y};
  const unsigned ee[10] = {e0.x, e0.y, e0.z, e0.w, e1.x, e1.y, e1.z, e1.w, e2.x, e2.y};
  unsigned o[10];
#pragma unroll
  for (int j = 0; j < 10; ++j) {
    const unsigned zz = (j & 1) ? Z[j >> 1].y : Z[j >> 1].x;
    const float b0 = acc[j >> 1][(j & 1) * 2] + blo(ee[j]), b1 = acc[j >> 1][(j & 1) * 2 + 1] + bhi(ee[j]);
    o[j] = pk2(gg * (blo(qq[j]) * blo(zz)) + og * b0, gg * (bhi(qq[j]) * bhi(zz)) + og * b1);
  }
  __builtin_amdgcn_raw_buffer_store_b128(u32x4{o[0], o[1], o[2], o[3]}, ro, qo, 0, 0);
  __builtin_amdgcn_raw_buffer_store_b128(u32x4{o[4], o[5], o[6], o[7]}, ro, qo + 64, 0, 0);
  __builtin_amdgcn_raw_buffer_store_b64(u32x2{o[8], o[9]}, ro, qo4, 0, 0);
  TS(21);
  if (b == 63 && w == 0) {
#pragma unroll 1
    for (int n = 0; n < 5; ++n) { const unsigned vo = prep_ + (F20 + n * 64 + lane) * FL_STR; flag_wait(c.rws, vo, flag_load(c.rws, vo), tag); }
    __builtin_amdgcn_raw_buffer_store_b64(tag, c.rws, lane == 0 ? 0u : OOB, OFF_E, AUX_SC1);
  }
}

extern "C" void kernel_launch(void* const* d_in, const int* in_sizes, int n_in, void* d_out, int out_size, void* d_ws, size_t ws_size, hipStream_t stream) {
  Params p;
  p.q = (const u16*)d_in[0]; p.k = (const u16*)d_in[1]; p.v = (const u16*)d_in[2]; p.sk = (const u16*)d_in[3];
  p.fk = (const u16*)d_in[4]; p.ssd = (const u16*)d_in[5]; p.w1 = (const u16*)d_in[7]; p.b1 = (const u16*)d_in[8];
  p.w2 = (const u16*)d_in[9]; p.b2 = (const u16*)d_in[10]; p.gw = (const u16*)d_in[11];
  p.out = (u16*)d_out; p.ws = (char*)d_ws;
  (void)hipFuncSetAttribute((const void*)k_fused, hipFuncAttributeMaxDynamicSharedMemorySize, LDS_BYTES);
  k_fused<<<256, 256, LDS_BYTES, stream>>>(p);
}
```
